```python
import torch

def _softmax(x, axis=-1):
    return torch.softmax(x, dim=axis)
def _gelu(x):
    return torch.nn.functional.gelu(x, approximate="tanh")
def _silu(x):
    return torch.nn.functional.silu(x)
def _sigmoid(x):
    return torch.sigmoid(x)
def _layernorm(x, axis=-1, eps=1e-5):
    m = x.mean(dim=axis, keepdim=True)
    v = x.var(dim=axis, keepdim=True, unbiased=False)
    return (x - m) / torch.sqrt(v + eps)
def _scatter_add(tgt, idx, src, axis=0):
    return tgt.clone().index_add_(axis, idx, src)
def _segment_sum(src, seg, n):
    out = torch.zeros((n,) + src.shape[1:], dtype=src.dtype)
    return out.index_add_(0, seg, src)
def _pad(x, pw):
    flat = tuple(v for pair in reversed(pw) for v in pair)
    return torch.nn.functional.pad(x, flat)

kernel_name = 'standalone__structured_sparse_n_m__ragged'

def setup_inputs():

    _w = (lambda a: (lambda t: t.to(torch.bfloat16) if t.is_floating_point() else t)(torch.as_tensor(a)))
    B, S, D = 16, 512, 1024
    assert D % 4 == 0, "d_model must be divisible by M=4 for 2:4 sparsity"
    return {
        "x": _w((lambda *s: torch.randn(*s, dtype=torch.bfloat16) * 0.05)(B, S, D)),
        "w": _w((lambda *s: torch.randn(*s, dtype=torch.bfloat16) * 0.05)(D, D)),
        "b": _w((lambda *s: torch.randn(*s, dtype=torch.bfloat16) * 0.05)(D)),
    }

def reference(x, w, b):
    B, S, D = x.shape
    N, M = 2, 4
    G = D // M
    wg = w.reshape(G, M, D)
    mag = torch.abs(wg)
    order = torch.argsort(mag, 1)
    thr = (lambda x, i, axis: torch.take_along_dim(x, i, dim=axis))(mag, order[:, M - N:M - N + 1, :], 1)
    mask = (lambda x, dt: x.to(dt))(mag >= thr, w.dtype)
    wp = (wg * mask).reshape(D, D)
    return torch.einsum("bsd,df->bsf", x, wp) + b
```

```cpp
#include <hip/hip_runtime.h>
#include <hip/hip_bf16.h>

typedef unsigned short u16;
typedef __attribute__((ext_vector_type(8))) __bf16 bf16x8;
typedef __attribute__((ext_vector_type(16))) __bf16 bf16x16;
typedef __attribute__((ext_vector_type(4))) float f32x4;
typedef __attribute__((ext_vector_type(4))) unsigned u32x4;
typedef __attribute__((ext_vector_type(8))) unsigned u32x8;
#define LAS __attribute__((address_space(3)))
#define GAS __attribute__((address_space(1)))

constexpr int MM = 8192, KK = 1024, NN = 1024;
constexpr size_t WS_WA = 0;
constexpr size_t WS_IA = 1u << 20;
constexpr size_t WS_CNT = WS_IA + (128u << 10);
constexpr size_t WS_EK = 2u << 20;
constexpr size_t WS_EA = 4u << 20;
constexpr int MAXR = 256;

__global__ __launch_bounds__(256) void prep_kernel(const u16* __restrict__ w, char* __restrict__ ws) {
    __shared__ __attribute__((aligned(16))) unsigned char sm[MAXR * 256 + MAXR * 32 + 64];
    u16* eaL = (u16*)sm;
    unsigned* ekL = (unsigned*)(sm + MAXR * 256);
    int* wtot = (int*)(sm + MAXR * 256 + MAXR * 32);
    const int T = blockIdx.x >> 2, Q = blockIdx.x & 3;
    const int tid = threadIdx.x, wv = tid >> 6, lane = tid & 63;
    const int g = T >> 2, j = T & 3;
    const int row = lane & 15, aq = lane >> 4;
    const int n = 64 * g + 32 * (j >> 1) + 8 * (row >> 2) + 4 * (j & 1) + (row & 3);
    const int s = Q * 4 + wv;
    const int k0 = 64 * s + 16 * aq;
    u16 v[16];
#pragma unroll
    for (int i = 0; i < 16; ++i) v[i] = w[(size_t)(k0 + i) * NN + n];
    unsigned outv[4];
    unsigned idx = 0;
    unsigned rest = 0;
#pragma unroll
    for (int gi = 0; gi < 4; ++gi) {
        const unsigned a0 = v[4 * gi], a1 = v[4 * gi + 1], a2 = v[4 * gi + 2], a3 = v[4 * gi + 3];
        const unsigned m0 = a0 & 0x7fffu, m1 = a1 & 0x7fffu, m2 = a2 & 0x7fffu, m3 = a3 & 0x7fffu;
        const unsigned h1 = max(m0, m1), l1 = min(m0, m1), h2 = max(m2, m3), l2 = min(m2, m3);
        unsigned thr = max(min(h1, h2), max(l1, l2));
        thr = max(thr, 1u);
        const unsigned f = (m0 >= thr ? 1u : 0u) | (m1 >= thr ? 2u : 0u) | (m2 >= thr ? 4u : 0u) | (m3 >= thr ? 8u : 0u);
        const unsigned f1 = f & (f - 1u);
        const unsigned f2 = f1 & (f1 - 1u);
        const unsigned i0 = f ? (unsigned)__builtin_ctz(f) : 0u;
        const unsigned i1 = f1 ? (unsigned)__builtin_ctz(f1) : 0u;
        const unsigned e0 = i0 == 0 ? a0 : (i0 == 1 ? a1 : (i0 == 2 ? a2 : a3));
        const unsigned e1 = i1 == 1 ? a1 : (i1 == 2 ? a2 : a3);
        outv[gi] = (f ? e0 : 0u) | ((f1 ? e1 : 0u) << 16);
        idx |= (i0 | (i1 << 2)) << (4 * gi);
        rest |= f2 << (4 * gi);
    }
    *(u32x4*)(ws + WS_WA + ((((size_t)g * 16 + s) * 4 + j) * 64 + lane) * 16) = u32x4{outv[0], outv[1], outv[2], outv[3]};
    *(u16*)(ws + WS_IA + (((size_t)g * 8 + (s >> 1)) * 64 + lane) * 16 + ((s & 1) * 2 + (j >> 1)) * 4 + (j & 1) * 2) = (u16)idx;

    const int c = __builtin_popcount(rest);
    int inc = c;
#pragma unroll
    for (int d = 1; d < 64; d <<= 1) {
        const int t = __shfl_up(inc, d);
        if (lane >= d) inc += t;
    }
    if (lane == 63) wtot[wv] = inc;
    __syncthreads();
    int base = 0, total = 0;
#pragma unroll
    for (int i = 0; i < 4; ++i) {
        const int t = wtot[i];
        if (i < wv) base += t;
        total += t;
    }
    const int R = (total + 7) >> 3;
    for (int u = tid; u < R * 16; u += 256) *(u32x4*)(sm + u * 16) = u32x4{0, 0, 0, 0};
    for (int u = tid; u < R * 8; u += 256) ekL[u] = 0;
    __syncthreads();
    int p = base + inc - c;
    unsigned rr = rest;
    while (rr) {
        const int e = __builtin_ctz(rr);
        rr &= rr - 1;
        u16 val = 0;
#pragma unroll
        for (int i = 0; i < 16; ++i) val = (i == e) ? v[i] : val;
        const int k = k0 + e;
        eaL[((p >> 3) * 16 + row) * 8 + (p & 7)] = val;
        ekL[p] = (unsigned)((k >> 7) * 16384 + ((k & 127) >> 3) * 16 + (k & 7) * 2);
        ++p;
    }
    __syncthreads();
    for (int u = tid; u < R * 16; u += 256) {
        const int r = u >> 4, rw = u & 15;
        *(u32x4*)(ws + WS_EA + (((size_t)T * MAXR + r) * 64 + Q * 16 + rw) * 16) = *(const u32x4*)(sm + u * 16);
    }
    for (int u = tid; u < R * 8; u += 256) {
        const int r = u >> 3, i = u & 7;
        *(unsigned*)(ws + WS_EK + ((((size_t)T * MAXR + r) * 4 + Q) * 8 + i) * 4) = ekL[u];
    }
    if (tid == 0) *(int*)(ws + WS_CNT + (T * 4 + Q) * 4) = total;
}

constexpr int XBYTES = 131072;
constexpr int LDS_BYTES = XBYTES + 64;
constexpr int PF = 4;

__device__ __forceinline__ unsigned pack_bf16(float lo, float hi) {
    typedef __attribute__((ext_vector_type(2))) __bf16 bf16x2;
    bf16x2 r;
    r[0] = (__bf16)lo;
    r[1] = (__bf16)hi;
    return __builtin_bit_cast(unsigned, r);
}

__global__ __launch_bounds__(512, 2) void main_kernel(const u16* __restrict__ x, const u16* __restrict__ bias,
                                                      const char* __restrict__ ws, u16* __restrict__ out) {
    extern __shared__ __attribute__((aligned(16))) unsigned char lds[];
    LAS unsigned char* L = (LAS unsigned char*)lds;
    volatile LAS unsigned* flags = (volatile LAS unsigned*)(L + XBYTES);
    const int tid = threadIdx.x, lane = tid & 63;
    const int wave = __builtin_amdgcn_readfirstlane(tid >> 6);
    const int b = blockIdx.x;
    const int mb = (b & 7) * 16 + (b >> 4), nh = (b >> 3) & 1;
    const int m0 = mb * 64;
    const int g = nh * 8 + wave;
    const int q = lane >> 4, m = lane & 15;

    if (tid < 4) flags[tid] = 0;
    asm volatile("s_waitcnt lgkmcnt(0)" ::: "memory");
    __builtin_amdgcn_s_barrier();

    const GAS u32x4* wa = (const GAS u32x4*)(ws + WS_WA + (size_t)g * 65536) + lane;
    const GAS u32x4* ia = (const GAS u32x4*)(ws + WS_IA + (size_t)g * 8192) + lane;
    u32x4 A[16][4];
    u32x4 I[8];
#pragma unroll
    for (int s = 0; s < PF; ++s) {
        if ((s & 1) == 0) I[s >> 1] = ia[(s >> 1) * 64];
#pragma unroll
        for (int j = 0; j < 4; ++j) A[s][j] = wa[(s * 4 + j) * 64];
    }
    f32x4 acc[4][4];
    {
        const u32x4 b0 = *(const GAS u32x4*)(bias + 64 * g + 8 * q);
        const u32x4 b1 = *(const GAS u32x4*)(bias + 64 * g + 32 + 8 * q);
        f32x4 init[4];
        init[0] = f32x4{__builtin_bit_cast(float, b0[0] << 16), __builtin_bit_cast(float, b0[0] & 0xffff0000u),
                        __builtin_bit_cast(float, b0[1] << 16), __builtin_bit_cast(float, b0[1] & 0xffff0000u)};
        init[1] = f32x4{__builtin_bit_cast(float, b0[2] << 16), __builtin_bit_cast(float, b0[2] & 0xffff0000u),
                        __builtin_bit_cast(float, b0[3] << 16), __builtin_bit_cast(float, b0[3] & 0xffff0000u)};
        init[2] = f32x4{__builtin_bit_cast(float, b1[0] << 16), __builtin_bit_cast(float, b1[0] & 0xffff0000u),
                        __builtin_bit_cast(float, b1[1] << 16), __builtin_bit_cast(float, b1[1] & 0xffff0000u)};
        init[3] = f32x4{__builtin_bit_cast(float, b1[2] << 16), __builtin_bit_cast(float, b1[2] & 0xffff0000u),
                        __builtin_bit_cast(float, b1[3] << 16), __builtin_bit_cast(float, b1[3] & 0xffff0000u)};
#pragma unroll
        for (int j = 0; j < 4; ++j)
#pragma unroll
            for (int i = 0; i < 4; ++i) acc[j][i] = init[j];
    }

    if (wave >= 4) {
        const int lw = wave - 4;
        const int rowl = lw * 16 + q;
#pragma unroll
        for (int c = 0; c < 8; ++c) {
#pragma unroll
            for (int p = 0; p < 4; ++p) {
                const int row = rowl + 4 * p;
                const u16* src = x + (size_t)(m0 + row) * KK + c * 128 + ((m ^ (row & 15)) * 8);
                __builtin_amdgcn_global_load_lds((const GAS unsigned*)src, (LAS unsigned*)(L + c * 16384 + (lw * 4 + p) * 1024), 16, 0, 0);
            }
        }
#define PUB(c, n)                                              \
    asm volatile("s_waitcnt vmcnt(" #n ")" ::: "memory");      \
    flags[lw] = c + 1;
        PUB(0, 28) PUB(1, 24) PUB(2, 20) PUB(3, 16) PUB(4, 12) PUB(5, 8) PUB(6, 4) PUB(7, 0)
#undef PUB
    }

    int ready = 0;
    auto wait_chunk = [&](int c) {
        if (ready <= c) {
            for (;;) {
                const u32x4 f = *(volatile LAS u32x4*)(L + XBYTES);
                const unsigned mn = min(min(f[0], f[1]), min(f[2], f[3]));
                ready = __builtin_amdgcn_readfirstlane((int)mn);
                if (ready > c) break;
                __builtin_amdgcn_s_sleep(1);
            }
        }
        asm volatile("" ::: "memory");
    };

    const int xb = m * 256 + ((q ^ m) * 16);
    auto loadB = [&](int s, bf16x16 (&B)[4]) {
#pragma unroll
        for (int i = 0; i < 4; ++i) {
            const int off = (s >> 1) * 16384 + i * 4096;
            const u32x4 lo = *(const LAS u32x4*)(L + ((xb ^ ((s & 1) * 128)) + off));
            const u32x4 hi = *(const LAS u32x4*)(L + ((xb ^ ((s & 1) * 128 + 64)) + off));
            const u32x8 t = __builtin_shufflevector(lo, hi, 0, 1, 2, 3, 4, 5, 6, 7);
            B[i] = __builtin_bit_cast(bf16x16, t);
        }
    };
    bf16x16 B[2][4];
    wait_chunk(0);
    loadB(0, B[0]);
#pragma unroll
    for (int s = 0; s < 16; ++s) {
        if (s + 1 < 16) {
            if (((s + 1) & 1) == 0) wait_chunk((s + 1) >> 1);
            loadB(s + 1, B[(s + 1) & 1]);
        }
        if (s + PF < 16) {
            if (((s + PF) & 1) == 0) I[(s + PF) >> 1] = ia[((s + PF) >> 1) * 64];
#pragma unroll
            for (int j = 0; j < 4; ++j) A[s + PF][j] = wa[((s + PF) * 4 + j) * 64];
        }
#pragma unroll
        for (int j = 0; j < 4; ++j) {
            const bf16x8 a = __builtin_bit_cast(bf16x8, A[s][j]);
            const int id = (int)I[s >> 1][(s & 1) * 2 + (j >> 1)];
#pragma unroll
            for (int i = 0; i < 4; ++i) {
                if (j & 1)
                    acc[j][i] = __builtin_amdgcn_smfmac_f32_16x16x64_bf16(a, B[s & 1][i], acc[j][i], id, 0, 1);
                else
                    acc[j][i] = __builtin_amdgcn_smfmac_f32_16x16x64_bf16(a, B[s & 1][i], acc[j][i], id, 0, 0);
            }
        }
    }

#pragma unroll
    for (int j = 0; j < 4; ++j) {
        const int T = g * 4 + j;
        const u32x4 cn = *(const GAS u32x4*)(ws + WS_CNT + T * 16);
        const int cmax = __builtin_amdgcn_readfirstlane((int)max(max(cn[0], cn[1]), max(cn[2], cn[3])));
        const int cq = (int)(q == 0 ? cn[0] : (q == 1 ? cn[1] : (q == 2 ? cn[2] : cn[3])));
        for (int r = 0; r * 8 < cmax; ++r) {
            u32x4 ea = u32x4{0, 0, 0, 0};
            u32x4 k0 = u32x4{0, 0, 0, 0}, k1 = u32x4{0, 0, 0, 0};
            if (r * 8 < cq) {
                ea = *(const GAS u32x4*)(ws + WS_EA + (((size_t)T * MAXR + r) * 64 + lane) * 16);
                const GAS u32x4* ek = (const GAS u32x4*)(ws + WS_EK + (((size_t)T * MAXR + r) * 4 + q) * 32);
                k0 = ek[0];
                k1 = ek[1];
            }
            int ad[8];
#pragma unroll
            for (int i = 0; i < 4; ++i) {
                ad[i] = (int)(k0[i] ^ (unsigned)(m * 16)) + m * 256;
                ad[4 + i] = (int)(k1[i] ^ (unsigned)(m * 16)) + m * 256;
            }
            const bf16x8 a = __builtin_bit_cast(bf16x8, ea);
#pragma unroll
            for (int i = 0; i < 4; ++i) {
                bf16x8 bb;
#pragma unroll
                for (int e = 0; e < 8; ++e) bb[e] = *(const LAS __bf16*)(L + ad[e] + i * 4096);
                acc[j][i] = __builtin_amdgcn_mfma_f32_16x16x32_bf16(a, bb, acc[j][i], 0, 0, 0);
            }
        }
    }

#pragma unroll
    for (int i = 0; i < 4; ++i) {
        u16* o = out + (size_t)(m0 + i * 16 + m) * NN + 64 * g + 8 * q;
#pragma unroll
        for (int p = 0; p < 2; ++p) {
            u32x4 r;
            r[0] = pack_bf16(acc[2 * p][i][0], acc[2 * p][i][1]);
            r[1] = pack_bf16(acc[2 * p][i][2], acc[2 * p][i][3]);
            r[2] = pack_bf16(acc[2 * p + 1][i][0], acc[2 * p + 1][i][1]);
            r[3] = pack_bf16(acc[2 * p + 1][i][2], acc[2 * p + 1][i][3]);
            *(GAS u32x4*)(o + 32 * p) = r;
        }
    }
}

extern "C" void kernel_launch(void* const* d_in, const int* in_sizes, int n_in, void* d_out, int out_size, void* d_ws,
                              size_t ws_size, hipStream_t stream) {
    const u16* x = (const u16*)d_in[0];
    const u16* w = (const u16*)d_in[1];
    const u16* b = (const u16*)d_in[2];
    (void)hipFuncSetAttribute((const void*)main_kernel, hipFuncAttributeMaxDynamicSharedMemorySize, LDS_BYTES);
    prep_kernel<<<256, 256, 0, stream>>>(w, (char*)d_ws);
    main_kernel<<<256, 512, LDS_BYTES, stream>>>(x, b, (const char*)d_ws, (u16*)d_out);
}
```

```cpp
#include <hip/hip_runtime.h>
#include <hip/hip_bf16.h>

typedef unsigned short u16;
typedef unsigned long long u64;
typedef __attribute__((ext_vector_type(8))) __bf16 bf16x8;
typedef __attribute__((ext_vector_type(16))) __bf16 bf16x16;
typedef __attribute__((ext_vector_type(4))) float f32x4;
typedef __attribute__((ext_vector_type(2))) unsigned u32x2;
typedef __attribute__((ext_vector_type(4))) unsigned u32x4;
typedef __attribute__((ext_vector_type(8))) unsigned u32x8;
#define LAS __attribute__((address_space(3)))
#define GAS __attribute__((address_space(1)))
#define RLX_AGENT __ATOMIC_RELAXED, __HIP_MEMORY_SCOPE_AGENT
#define SC1 16

#define X_AUX 2
#define PF 4
#define POLL_SLEEP 4
#define SE 15
#define XFLY 8

constexpr int MM = 8192, KK = 1024, NN = 1024;
constexpr unsigned WS_EPOCH = 0;
constexpr unsigned WS_FLAG = 4096;
constexpr unsigned WS_WA = 1u << 20;
constexpr unsigned WS_IA = 2u << 20;
constexpr unsigned WS_E = 3u << 20;
constexpr unsigned WS_BYTES = 7u << 20;
constexpr int ECAP = 2048;
constexpr int NREP = 16;
constexpr int L_X = 0, L_TR = 131072, L_IB = L_TR + 4096, L_SY = L_IB + 1024, L_CN = L_SY + 64, L_EC = L_CN + 512,
              LDS_BYTES = L_EC + 8 * 2048;
constexpr int SPIN_CAP = 1 << 21;
constexpr int POLL_CAP = 1 << 17;

template <int V> struct IC { static constexpr int value = V; };
__device__ __forceinline__ unsigned pack_bf16(float lo, float hi) {
    typedef __attribute__((ext_vector_type(2))) __bf16 bf16x2;
    bf16x2 r;
    r[0] = (__bf16)lo;
    r[1] = (__bf16)hi;
    return __builtin_bit_cast(unsigned, r);
}
typedef __attribute__((ext_vector_type(2))) unsigned short us2;
typedef __attribute__((ext_vector_type(2))) short ss2;
__device__ __forceinline__ unsigned pkmax(unsigned a, unsigned b) {
    return __builtin_bit_cast(unsigned, __builtin_elementwise_max(__builtin_bit_cast(us2, a), __builtin_bit_cast(us2, b)));
}
__device__ __forceinline__ unsigned pkmin(unsigned a, unsigned b) {
    return __builtin_bit_cast(unsigned, __builtin_elementwise_min(__builtin_bit_cast(us2, a), __builtin_bit_cast(us2, b)));
}
__device__ __forceinline__ unsigned pklt(unsigned a, unsigned b) {
    const ss2 d = __builtin_bit_cast(ss2, a) - __builtin_bit_cast(ss2, b);
    unsigned r = __builtin_bit_cast(unsigned, (ss2)(d >> (short)15));
    asm("" : "+v"(r));
    return r;
}
__device__ __forceinline__ unsigned bsel(unsigned m, unsigned a, unsigned b) { return (m & a) | (~m & b); }

__device__ __forceinline__ void extras_round(LAS unsigned char* L, const u32x4 ea0, const u32x4 ea1, const u32x4 ev0, const u32x4 ev1, int m, f32x4 (&acc)[4]) {
    unsigned ad[8], vv[8];
#pragma unroll
    for (int i = 0; i < 8; ++i) {
        const unsigned a = i < 4 ? ea0[i] : ea1[i - 4];
        const unsigned v = i < 4 ? ev0[i] : ev1[i - 4];
        ad[i] = (a ^ (unsigned)(m * 16)) + (unsigned)(m * 256);
        vv[i] = ((u16)v == (u16)m) ? v : 0u;
    }
    u32x4 bw[4];
#pragma unroll
    for (int i = 0; i < 4; ++i)
#pragma unroll
        for (int p = 0; p < 4; ++p) {
            const unsigned lo = *(const LAS u16*)(L + ad[2 * p] + i * 4096);
            const unsigned hi = *(const LAS u16*)(L + ad[2 * p + 1] + i * 4096);
            bw[i][p] = lo | (hi << 16);
        }
    const bf16x8 a = __builtin_bit_cast(bf16x8, u32x4{__builtin_amdgcn_perm(vv[1], vv[0], 0x07060302u), __builtin_amdgcn_perm(vv[3], vv[2], 0x07060302u),
                                                      __builtin_amdgcn_perm(vv[5], vv[4], 0x07060302u), __builtin_amdgcn_perm(vv[7], vv[6], 0x07060302u)});
#pragma unroll
    for (int i = 0; i < 4; ++i) acc[i] = __builtin_amdgcn_mfma_f32_16x16x32_bf16(a, __builtin_bit_cast(bf16x8, bw[i]), acc[i], 0, 0, 0);
}

__global__ __launch_bounds__(512, 2) void fused_kernel(const u16* __restrict__ x, const u16* __restrict__ w,
                                                       const u16* __restrict__ bias, char* ws, u16* __restrict__ out) {
    extern __shared__ __attribute__((aligned(16))) unsigned char lds[];
    LAS unsigned char* L = (LAS unsigned char*)lds;
    volatile LAS unsigned* sy = (volatile LAS unsigned*)(L + L_SY);
    const int tid = threadIdx.x, lane = tid & 63;
    const int wave = __builtin_amdgcn_readfirstlane(tid >> 6);
    const int b = blockIdx.x;
    const int mb = (b & 7) * 16 + (b >> 4), nh = (b >> 3) & 1;
    const int m0 = mb * 64;
    const int g = nh * 8 + wave;
    const int q = lane >> 4, m = lane & 15;
    const __amdgpu_buffer_rsrc_t rs = __builtin_amdgcn_make_buffer_rsrc(ws, 0, WS_BYTES, 0x00020000);

    if (wave < 4) {
        const int combo = (b & 7) + 8 * (b >> 5), pj = (b >> 3) & 3;
        const int pg = combo >> 2, pQ = combo & 3;
        const int aq = wave;
        const int sl = lane >> 4, run = (lane >> 2) & 3, gi = lane & 3;
        const int n0 = 64 * pg + 32 * (pj >> 1) + 8 * run + 4 * (pj & 1);
        const int k0 = 256 * pQ + 64 * sl + 16 * aq + 4 * gi;
        u32x2 v[4];
#pragma unroll
        for (int i = 0; i < 4; ++i) v[i] = *(const GAS u32x2*)(w + (size_t)(k0 + i) * NN + n0);
        u64 ep = 0;
        if (wave == 0) ep = __hip_atomic_load((GAS u64*)(ws + WS_EPOCH), RLX_AGENT);
        if (tid < 16) sy[tid] = 0;
        asm volatile("s_waitcnt lgkmcnt(0)" ::: "memory");
        __builtin_amdgcn_s_barrier();
        asm volatile("" : "+v"(v[0]), "+v"(v[1]), "+v"(v[2]), "+v"(v[3])::"memory");
        LAS unsigned* trL = (LAS unsigned*)(L + L_TR + aq * 1024) + (sl * 16 + 4 * run) * 4 + gi;
        LAS unsigned char* ibL = L + L_IB + aq * 256;
        LAS unsigned char* ibW = ibL + 4 * run * 16 + sl * 4 + gi;
        unsigned rest = 0;
#pragma unroll
        for (int p = 0; p < 2; ++p) {
            const unsigned r0 = v[0][p], r1 = v[1][p], r2 = v[2][p], r3 = v[3][p];
            const unsigned a0 = r0 & 0x7fff7fffu, a1 = r1 & 0x7fff7fffu, a2 = r2 & 0x7fff7fffu, a3 = r3 & 0x7fff7fffu;
            const unsigned h1 = pkmax(a0, a1), l1 = pkmin(a0, a1), h2 = pkmax(a2, a3), l2 = pkmin(a2, a3);
            const unsigned thr0 = pkmax(pkmin(h1, h2), pkmax(l1, l2));
            const unsigned thr = pkmax(thr0, 0x00010001u);
            const unsigned D0 = pklt(a0, thr), D1 = pklt(a1, thr), D2 = pklt(a2, thr), D3 = pklt(a3, thr);
            const unsigned Z = pklt(thr0, 0x00010001u);
            const unsigned e0 = bsel(D0, bsel(D1, bsel(D2, r3 & ~D3, r2), r1), r0);
            const unsigned e1 = bsel(D3, bsel(D2, r1, r2), r3) & ~Z;
            const unsigned ia = D0 & 0x00010001u, ib = ia & D1, ic = ib & D2;
            const unsigned ja = D3 & 0x00040004u, jb = ja & D2;
            const unsigned nib = (ia + ib + ic) + (0x000c000cu - ja - jb);
            const unsigned X1 = ~D1 & ~D0 & ~(D2 & D3), X2 = ~D2 & ~D3 & ~(D0 & D1);
            rest |= ((X1 & 0x00010001u) | (X2 & 0x00020002u)) << (2 * p);
            trL[(2 * p) * 4] = __builtin_amdgcn_perm(e1, e0, 0x05040100u);
            trL[(2 * p + 1) * 4] = __builtin_amdgcn_perm(e1, e0, 0x07060302u);
            ibW[(2 * p) * 16] = (unsigned char)nib;
            ibW[(2 * p + 1) * 16] = (unsigned char)(nib >> 16);
        }
        const int cnt = __builtin_popcount(rest);
        const u64 bal0 = __ballot(cnt & 1);
        int pre = __builtin_amdgcn_mbcnt_hi((unsigned)(bal0 >> 32), __builtin_amdgcn_mbcnt_lo((unsigned)bal0, 0));
        int wsum = __builtin_popcountll(bal0);
        if (__ballot(cnt > 1)) {
#pragma unroll
            for (int bit = 1; bit < 4; ++bit) {
                const u64 bal = __ballot((cnt >> bit) & 1);
                pre += __builtin_amdgcn_mbcnt_hi((unsigned)(bal >> 32), __builtin_amdgcn_mbcnt_lo((unsigned)bal, 0)) << bit;
                wsum += __builtin_popcountll(bal) << bit;
            }
        }
        if (lane == 0) sy[4 + aq] = (unsigned)wsum + 1u;
        if (wave == 0 && lane == 0) {
            sy[8] = (unsigned)ep;
            sy[9] = (unsigned)(ep >> 32);
        }
        asm volatile("s_waitcnt lgkmcnt(0)" ::: "memory");
        {
            const u32x4 d = *(const LAS u32x4*)(L + L_TR + aq * 1024 + lane * 16);
            const u32x4 ib = *(const LAS u32x4*)(ibL + (lane & 15) * 16);
            __builtin_amdgcn_raw_buffer_store_b128(d, rs, WS_WA + (((pg * 16 + 4 * pQ + sl) * 4 + pj) * 64 + aq * 16 + (lane & 15)) * 16, 0, SC1);
            if (lane < 16) {
                unsigned h[4];
#pragma unroll
                for (int s = 0; s < 4; ++s) h[s] = ib[s] | (ib[s] >> 4);
                __builtin_amdgcn_raw_buffer_store_b64(u32x2{__builtin_amdgcn_perm(h[1], h[0], 0x06040200u), __builtin_amdgcn_perm(h[3], h[2], 0x06040200u)}, rs,
                                                      WS_IA + (((pg * 4 + pQ) * 64 + aq * 16 + lane) * 4 + pj) * 8, 0, SC1);
            }
        }
        u32x4 wt;
        for (int sp = 0; sp < SPIN_CAP; ++sp) {
            wt = *(volatile LAS u32x4*)(L + L_SY + 16);
            if (wt[0] && wt[1] && wt[2] && wt[3]) break;
        }
        int base = 0, total = 0;
#pragma unroll
        for (int i = 0; i < 4; ++i) {
            const int t = (int)wt[i] - 1;
            if (i < aq) base += t;
            total += t;
        }
        total = __builtin_amdgcn_readfirstlane(total);
        const int ebase = WS_E + ((pg * 4 + pj) * 4 + pQ) * ECAP * 8;
        int p = base + pre;
        unsigned rr = rest;
        while (rr) {
            const int e = __builtin_ctz(rr);
            rr &= rr - 1;
            const int h = e >> 4, pp = (e >> 1) & 1, t = e & 1;
            const u32x2 vv = t ? v[2] : v[1];
            const unsigned val = ((pp ? vv[1] : vv[0]) >> (h * 16)) & 0xffffu;
            const int k = k0 + 1 + t;
            __builtin_amdgcn_raw_buffer_store_b64(u32x2{(unsigned)((k >> 7) * 16384 + (k & 127) * 2), (val << 16) | (unsigned)(4 * run + 2 * pp + h)}, rs,
                                                  ebase + p * 8, 0, SC1);
            ++p;
        }
        asm volatile("s_waitcnt vmcnt(0)" ::: "memory");
        unsigned old = 0;
        if (lane == 0) old = __hip_atomic_fetch_add((LAS unsigned*)(L + L_SY + 8), 1u, __ATOMIC_RELAXED, __HIP_MEMORY_SCOPE_WORKGROUP);
        old = __builtin_amdgcn_readfirstlane(old);
        if (old == 3) {
            const u64 e = ((u64)sy[9] << 32) | sy[8];
            const u64 fl = ((e + 1ull) << 12) | (u64)total;
            if (lane < NREP) __hip_atomic_store((GAS u64*)(ws + WS_FLAG + ((lane * 16 + pg) * 16 + pj * 4 + pQ) * 8), fl, RLX_AGENT);
        }
    } else {
        __builtin_amdgcn_s_barrier();
        const int lw = wave - 4;
        const int rowl = lw * 16 + q;
#pragma unroll
        for (int c = 0; c < 8; ++c) {
#pragma unroll
            for (int p = 0; p < 4; ++p) {
                const int row = rowl + 4 * p;
                const u16* src = x + (size_t)(m0 + row) * KK + c * 128 + ((m ^ (row & 15)) * 8);
                __builtin_amdgcn_global_load_lds((const GAS unsigned*)src, (LAS unsigned*)(L + c * 16384 + (lw * 4 + p) * 1024), 16, 0, X_AUX);
                if (c * 4 + p >= XFLY) asm volatile("s_waitcnt vmcnt(%0)" ::"i"(XFLY) : "memory");
            }
        }
        asm volatile("s_waitcnt vmcnt(0)" ::: "memory");
        if (lane == 0) __hip_atomic_fetch_add((LAS unsigned*)(L + L_SY), 1u, __ATOMIC_RELAXED, __HIP_MEMORY_SCOPE_WORKGROUP);
    }

    f32x4 acc[4][4];
    {
        const u32x4 b0 = *(const GAS u32x4*)(bias + 64 * g + 8 * q);
        const u32x4 b1 = *(const GAS u32x4*)(bias + 64 * g + 32 + 8 * q);
        f32x4 init[4];
        init[0] = f32x4{__builtin_bit_cast(float, b0[0] << 16), __builtin_bit_cast(float, b0[0] & 0xffff0000u),
                        __builtin_bit_cast(float, b0[1] << 16), __builtin_bit_cast(float, b0[1] & 0xffff0000u)};
        init[1] = f32x4{__builtin_bit_cast(float, b0[2] << 16), __builtin_bit_cast(float, b0[2] & 0xffff0000u),
                        __builtin_bit_cast(float, b0[3] << 16), __builtin_bit_cast(float, b0[3] & 0xffff0000u)};
        init[2] = f32x4{__builtin_bit_cast(float, b1[0] << 16), __builtin_bit_cast(float, b1[0] & 0xffff0000u),
                        __builtin_bit_cast(float, b1[1] << 16), __builtin_bit_cast(float, b1[1] & 0xffff0000u)};
        init[3] = f32x4{__builtin_bit_cast(float, b1[2] << 16), __builtin_bit_cast(float, b1[2] & 0xffff0000u),
                        __builtin_bit_cast(float, b1[3] << 16), __builtin_bit_cast(float, b1[3] & 0xffff0000u)};
#pragma unroll
        for (int j = 0; j < 4; ++j)
#pragma unroll
            for (int i = 0; i < 4; ++i) acc[j][i] = init[j];
    }
    LAS unsigned char* EC = L + L_EC + wave * 2048;
    *(LAS u32x4*)(EC + lane * 16) = u32x4{0, 0, 0, 0};
    *(LAS u32x4*)(EC + 1024 + lane * 16) = u32x4{0, 0, 0, 0};

    u64 tag = 0;
    if (wave == 0) {
        for (int sp = 0; sp < SPIN_CAP; ++sp) {
            if (sy[0] == 4u) break;
            __builtin_amdgcn_s_sleep(1);
        }
        tag = ((((u64)sy[9] << 32) | sy[8]) + 1ull) & ((1ull << 52) - 1ull);
        const int foff = WS_FLAG + ((mb & (NREP - 1)) * 16 + nh * 8) * 128 + lane * 16;
        u32x4 f;
        for (int sp = 0; sp < POLL_CAP; ++sp) {
            f = __builtin_amdgcn_raw_buffer_load_b128(rs, foff, 0, SC1);
            asm volatile("" ::: "memory");
            const u64 f0 = ((u64)f[1] << 32) | f[0], f1 = ((u64)f[3] << 32) | f[2];
            if (__all((f0 >> 12) == tag && (f1 >> 12) == tag)) break;
            __builtin_amdgcn_s_sleep(POLL_SLEEP);
        }
        *(LAS u32x2*)(L + L_CN + lane * 8) = u32x2{f[0] & 0xfffu, f[2] & 0xfffu};
        asm volatile("s_waitcnt lgkmcnt(0)" ::: "memory");
        if (lane == 0) sy[3] = 1u;
    } else {
        for (int sp = 0; sp < SPIN_CAP; ++sp) {
            if (sy[3]) break;
            __builtin_amdgcn_s_sleep(1);
        }
    }
    __builtin_amdgcn_fence(__ATOMIC_ACQUIRE, "wavefront");
    asm volatile("" ::: "memory");

    const int eQ = (lane >> 2) & 3, eu = lane & 3;
    const int eoff = WS_E + ((g * 4 + q) * 4 + eQ) * ECAP * 8 + eu * 32;
    const u32x4 er0 = __builtin_amdgcn_raw_buffer_load_b128(rs, eoff, 0, SC1);
    const u32x4 er1 = __builtin_amdgcn_raw_buffer_load_b128(rs, eoff + 16, 0, SC1);
    const int waoff = WS_WA + g * 65536, iaoff = WS_IA + g * 8192;
    const int rot = (b >> 4) & 3;
    u32x4 A[16][4];
    u32x4 I[4][2];
    auto loadA = [&](int s) {
        if ((s & 3) == 0) {
            I[s >> 2][0] = __builtin_amdgcn_raw_buffer_load_b128(rs, lane * 32, iaoff + (((s >> 2) + rot) & 3) * 2048, SC1);
            I[s >> 2][1] = __builtin_amdgcn_raw_buffer_load_b128(rs, lane * 32 + 16, iaoff + (((s >> 2) + rot) & 3) * 2048, SC1);
        }
#pragma unroll
        for (int j = 0; j < 4; ++j) A[s][j] = __builtin_amdgcn_raw_buffer_load_b128(rs, lane * 16 + j * 1024, waoff + ((((s >> 2) + rot) & 3) * 4 + (s & 3)) * 4096, SC1);
    };
#pragma unroll
    for (int s = 0; s < PF; ++s) loadA(s);
    __builtin_amdgcn_sched_barrier(0);

    auto do_extras = [&]() {
    const u32x4 cn = *(const volatile LAS u32x4*)(L + L_CN + (wave * 16 + q * 4) * 4);
    const unsigned ctot = cn[0] + cn[1] + cn[2] + cn[3];
    const unsigned cbig = max(max(cn[0], cn[1]), max(cn[2], cn[3]));
    const bool irregular = cbig > 16u;
    {
        const unsigned cme = eQ == 0 ? cn[0] : (eQ == 1 ? cn[1] : (eQ == 2 ? cn[2] : cn[3]));
        const unsigned pre = (eQ > 0 ? cn[0] : 0u) + (eQ > 1 ? cn[1] : 0u) + (eQ > 2 ? cn[2] : 0u);
#pragma unroll
        for (int e = 0; e < 4; ++e) {
            const unsigned idx = (unsigned)(4 * eu + e);
            const unsigned sg = pre + idx;
            if (idx < cme && !irregular) {
                LAS unsigned* dst = (LAS unsigned*)(EC + q * 512 + (sg >> 5) * 256 + (sg & 3) * 32 + ((sg >> 2) & 7) * 4);
                dst[0] = e < 2 ? er0[2 * e] : er1[2 * e - 4];
                dst[32] = e < 2 ? er0[2 * e + 1] : er1[2 * e - 3];
            }
        }
    }
    asm volatile("s_waitcnt lgkmcnt(0)" ::: "memory");
    {
        unsigned ad[4][8];
        u32x4 af[4];
#pragma unroll
        for (int j = 0; j < 4; ++j) {
            const LAS u32x4* t = (const LAS u32x4*)(EC + j * 512 + q * 32);
            const u32x4 a0 = t[0], a1 = t[1], v0 = t[8], v1 = t[9];
            unsigned vv[8];
#pragma unroll
            for (int i = 0; i < 8; ++i) {
                const unsigned a = i < 4 ? a0[i] : a1[i - 4];
                const unsigned v = i < 4 ? v0[i] : v1[i - 4];
                ad[j][i] = (a ^ (unsigned)(m * 16)) + (unsigned)(m * 256);
                vv[i] = ((u16)v == (u16)m) ? v : 0u;
            }
            af[j] = u32x4{__builtin_amdgcn_perm(vv[1], vv[0], 0x07060302u), __builtin_amdgcn_perm(vv[3], vv[2], 0x07060302u),
                          __builtin_amdgcn_perm(vv[5], vv[4], 0x07060302u), __builtin_amdgcn_perm(vv[7], vv[6], 0x07060302u)};
        }
        __builtin_amdgcn_sched_barrier(0);
        unsigned gw[2][4][8];
        auto gather = [&](int j) {
#pragma unroll
            for (int i = 0; i < 4; ++i)
#pragma unroll
                for (int e = 0; e < 8; ++e) gw[j & 1][i][e] = *(const LAS u16*)(L + ad[j][e] + i * 4096);
        };
        gather(0);
#pragma unroll
        for (int j = 0; j < 4; ++j) {
            if (j + 1 < 4) gather(j + 1);
            __builtin_amdgcn_sched_barrier(0);
#pragma unroll
            for (int i = 0; i < 4; ++i) {
                u32x4 bw;
#pragma unroll
                for (int p = 0; p < 4; ++p) bw[p] = gw[j & 1][i][2 * p] | (gw[j & 1][i][2 * p + 1] << 16);
                acc[j][i] = __builtin_amdgcn_mfma_f32_16x16x32_bf16(__builtin_bit_cast(bf16x8, af[j]), __builtin_bit_cast(bf16x8, bw), acc[j][i], 0, 0, 0);
            }
            __builtin_amdgcn_sched_barrier(0);
        }
    }
    if (__any(ctot > 32u || irregular)) {
#pragma unroll
        for (int j = 0; j < 4; ++j) {
            const int ct = __builtin_amdgcn_readlane((int)ctot, 16 * j), cb = __builtin_amdgcn_readlane((int)cbig, 16 * j);
            if (cb > 16) {
                const int cq = (int)*(const volatile LAS unsigned*)(L + L_CN + (wave * 16 + j * 4 + q) * 4);
                const int lb = WS_E + ((g * 4 + j) * 4 + q) * ECAP * 8;
                for (int r = 0; r * 8 < cb; ++r) {
                    u32x4 e[4];
#pragma unroll
                    for (int u = 0; u < 4; ++u) e[u] = __builtin_amdgcn_raw_buffer_load_b128(rs, lb + min(r * 64 + u * 16, ECAP * 8 - 16), 0, SC1);
                    u32x4 a0, a1, v0, v1;
#pragma unroll
                    for (int i = 0; i < 4; ++i) {
                        const bool ok0 = r * 8 + i < cq, ok1 = r * 8 + 4 + i < cq;
                        a0[i] = ok0 ? e[i >> 1][(i & 1) * 2] : 0u;
                        v0[i] = ok0 ? e[i >> 1][(i & 1) * 2 + 1] : 0u;
                        a1[i] = ok1 ? e[2 + (i >> 1)][(i & 1) * 2] : 0u;
                        v1[i] = ok1 ? e[2 + (i >> 1)][(i & 1) * 2 + 1] : 0u;
                    }
                    extras_round(L, a0, a1, v0, v1, m, acc[j]);
                }
            } else if (ct > 32) {
                const LAS u32x4* t = (const LAS u32x4*)(EC + j * 512 + 256 + q * 32);
                extras_round(L, t[0], t[1], t[8], t[9], m, acc[j]);
            }
        }
    }

    };
    const int xb = m * 256 + ((q ^ m) * 16);
    auto loadB = [&](int s, bf16x16 (&B)[4]) {
#pragma unroll
        for (int i = 0; i < 4; ++i) {
            const int off = ((s >> 1) & 1) * 16384 + i * 4096;
            const int qb = (((s >> 2) + rot) & 3) * 32768;
            const u32x4 lo = *(const LAS u32x4*)(L + ((xb ^ ((s & 1) * 128)) + qb + off));
            const u32x4 hi = *(const LAS u32x4*)(L + ((xb ^ ((s & 1) * 128 + 64)) + qb + off));
            const u32x8 t = __builtin_shufflevector(lo, hi, 0, 1, 2, 3, 4, 5, 6, 7);
            B[i] = __builtin_bit_cast(bf16x16, t);
        }
    };
    bf16x16 B[2][4];
    loadB(0, B[0]);
    __builtin_amdgcn_sched_barrier(0);
    auto step = [&](auto sc) {
        constexpr int s = decltype(sc)::value;
        if constexpr (s + 1 < 16 && s != SE) loadB(s + 1, B[(s + 1) & 1]);
        if constexpr (s + PF < 16) loadA(s + PF);
#pragma unroll
        for (int j = 0; j < 4; ++j) {
            const bf16x8 a = __builtin_bit_cast(bf16x8, A[s][j]);
            const int id = (int)I[s >> 2][j >> 1][(j & 1) * 2 + ((s & 3) >> 1)];
#pragma unroll
            for (int i = 0; i < 4; ++i) acc[j][i] = __builtin_amdgcn_smfmac_f32_16x16x64_bf16(a, B[s & 1][i], acc[j][i], id, 0, s & 1);
        }
#pragma unroll
        for (int u = 0; u < 8; ++u) {
            __builtin_amdgcn_sched_group_barrier(0x8, 1, 0);
            __builtin_amdgcn_sched_group_barrier(0x100, 1, 0);
        }
#pragma unroll
        for (int u = 0; u < 6; ++u) {
            __builtin_amdgcn_sched_group_barrier(0x8, 1, 0);
            __builtin_amdgcn_sched_group_barrier(0x20, 1, 0);
        }
        __builtin_amdgcn_sched_group_barrier(0x8, 2, 0);
        __builtin_amdgcn_sched_barrier(0);
        if constexpr (s == SE) {
            do_extras();
            if constexpr (s + 1 < 16) loadB(s + 1, B[(s + 1) & 1]);
            __builtin_amdgcn_sched_barrier(0);
        }
    };
#define ST(n) step(IC<n>{});
    ST(0) ST(1) ST(2) ST(3) ST(4) ST(5) ST(6) ST(7) ST(8) ST(9) ST(10) ST(11) ST(12) ST(13) ST(14) ST(15)
#undef ST

#pragma unroll
    for (int i = 0; i < 4; ++i) {
        u16* o = out + (size_t)(m0 + i * 16 + m) * NN + 64 * g + 8 * q;
#pragma unroll
        for (int p = 0; p < 2; ++p) {
            u32x4 r;
            r[0] = pack_bf16(acc[2 * p][i][0], acc[2 * p][i][1]);
            r[1] = pack_bf16(acc[2 * p][i][2], acc[2 * p][i][3]);
            r[2] = pack_bf16(acc[2 * p + 1][i][0], acc[2 * p + 1][i][1]);
            r[3] = pack_bf16(acc[2 * p + 1][i][2], acc[2 * p + 1][i][3]);
            *(GAS u32x4*)(o + 32 * p) = r;
        }
    }

    if (b == 0 && wave == 0) {
        GAS u64* fp = (GAS u64*)(ws + WS_FLAG) + lane;
        for (int sp = 0; sp < POLL_CAP; ++sp) {
            bool ok = true;
#pragma unroll
            for (int u = 0; u < 4; ++u) ok &= (__hip_atomic_load(fp + 64 * u, RLX_AGENT) >> 12) == tag;
            if (__all(ok)) break;
            __builtin_amdgcn_s_sleep(2);
        }
        if (lane == 0) __hip_atomic_store((GAS u64*)(ws + WS_EPOCH), (((u64)sy[9] << 32) | sy[8]) + 1ull, RLX_AGENT);
    }
}

extern "C" void kernel_launch(void* const* d_in, const int* in_sizes, int n_in, void* d_out, int out_size, void* d_ws,
                              size_t ws_size, hipStream_t stream) {
    const u16* x = (const u16*)d_in[0];
    const u16* w = (const u16*)d_in[1];
    const u16* b = (const u16*)d_in[2];
    (void)hipFuncSetAttribute((const void*)fused_kernel, hipFuncAttributeMaxDynamicSharedMemorySize, LDS_BYTES);
    fused_kernel<<<256, 512, LDS_BYTES, stream>>>(x, w, b, (char*)d_ws, (u16*)d_out);
}
```

```cpp
#include <hip/hip_runtime.h>
#include <hip/hip_bf16.h>

typedef unsigned short u16;
typedef unsigned long long u64;
typedef __attribute__((ext_vector_type(8))) __bf16 bf16x8;
typedef __attribute__((ext_vector_type(16))) __bf16 bf16x16;
typedef __attribute__((ext_vector_type(4))) float f32x4;
typedef __attribute__((ext_vector_type(2))) unsigned u32x2;
typedef __attribute__((ext_vector_type(4))) unsigned u32x4;
typedef __attribute__((ext_vector_type(8))) unsigned u32x8;
#define LAS __attribute__((address_space(3)))
#define GAS __attribute__((address_space(1)))
#define RLX_AGENT __ATOMIC_RELAXED, __HIP_MEMORY_SCOPE_AGENT
#define SC1 16

#define X_AUX 2
#define PF 3
#define POLL_SLEEP 4
#define ST_AUX 18
#define XFLY 8

constexpr int MM = 8192, KK = 1024, NN = 1024;
constexpr unsigned WS_EPOCH = 0;
constexpr unsigned WS_FLAG = 4096;
constexpr unsigned WS_WA = 1u << 20;
constexpr unsigned WS_IA = 2u << 20;
constexpr unsigned WS_E = 3u << 20;
constexpr unsigned WS_BYTES = 7u << 20;
constexpr int ECAP = 2048;
constexpr int NREP = 16;
constexpr int L_X = 0, L_TR = 131072, L_IB = L_TR + 4096, L_SY = L_IB + 1024, L_CN = L_SY + 64, L_EC = L_CN + 512,
              LDS_BYTES = L_EC + 8 * 2048;
constexpr int SPIN_CAP = 1 << 21;
constexpr int POLL_CAP = 1 << 17;

template <int V> struct IC { static constexpr int value = V; };
__device__ __forceinline__ unsigned pack_bf16(float lo, float hi) {
    typedef __attribute__((ext_vector_type(2))) __bf16 bf16x2;
    bf16x2 r;
    r[0] = (__bf16)lo;
    r[1] = (__bf16)hi;
    return __builtin_bit_cast(unsigned, r);
}
typedef __attribute__((ext_vector_type(2))) unsigned short us2;
typedef __attribute__((ext_vector_type(2))) short ss2;
__device__ __forceinline__ unsigned pkmax(unsigned a, unsigned b) {
    return __builtin_bit_cast(unsigned, __builtin_elementwise_max(__builtin_bit_cast(us2, a), __builtin_bit_cast(us2, b)));
}
__device__ __forceinline__ unsigned pkmin(unsigned a, unsigned b) {
    return __builtin_bit_cast(unsigned, __builtin_elementwise_min(__builtin_bit_cast(us2, a), __builtin_bit_cast(us2, b)));
}
__device__ __forceinline__ unsigned pklt(unsigned a, unsigned b) {
    const ss2 d = __builtin_bit_cast(ss2, a) - __builtin_bit_cast(ss2, b);
    unsigned r = __builtin_bit_cast(unsigned, (ss2)(d >> (short)15));
    asm("" : "+v"(r));
    return r;
}
__device__ __forceinline__ unsigned bsel(unsigned m, unsigned a, unsigned b) { return (m & a) | (~m & b); }

__device__ __forceinline__ void extras_round(LAS unsigned char* L, const u32x4 ea0, const u32x4 ea1, const u32x4 ev0, const u32x4 ev1, int m, f32x4 (&acc)[4]) {
    unsigned ad[8], vv[8];
#pragma unroll
    for (int i = 0; i < 8; ++i) {
        const unsigned a = i < 4 ? ea0[i] : ea1[i - 4];
        const unsigned v = i < 4 ? ev0[i] : ev1[i - 4];
        ad[i] = (a ^ (unsigned)(m * 16)) + (unsigned)(m * 256);
        vv[i] = ((u16)v == (u16)m) ? v : 0u;
    }
    u32x4 bw[4];
#pragma unroll
    for (int i = 0; i < 4; ++i)
#pragma unroll
        for (int p = 0; p < 4; ++p) {
            const unsigned lo = *(const LAS u16*)(L + ad[2 * p] + i * 4096);
            const unsigned hi = *(const LAS u16*)(L + ad[2 * p + 1] + i * 4096);
            bw[i][p] = lo | (hi << 16);
        }
    const bf16x8 a = __builtin_bit_cast(bf16x8, u32x4{__builtin_amdgcn_perm(vv[1], vv[0], 0x07060302u), __builtin_amdgcn_perm(vv[3], vv[2], 0x07060302u),
                                                      __builtin_amdgcn_perm(vv[5], vv[4], 0x07060302u), __builtin_amdgcn_perm(vv[7], vv[6], 0x07060302u)});
#pragma unroll
    for (int i = 0; i < 4; ++i) acc[i] = __builtin_amdgcn_mfma_f32_16x16x32_bf16(a, __builtin_bit_cast(bf16x8, bw[i]), acc[i], 0, 0, 0);
}

__global__ __launch_bounds__(512, 2) void fused_kernel(const u16* __restrict__ x, const u16* __restrict__ w,
                                                       const u16* __restrict__ bias, char* ws, u16* __restrict__ out) {
    extern __shared__ __attribute__((aligned(16))) unsigned char lds[];
    LAS unsigned char* L = (LAS unsigned char*)lds;
    volatile LAS unsigned* sy = (volatile LAS unsigned*)(L + L_SY);
    const int tid = threadIdx.x, lane = tid & 63;
    const int wave = __builtin_amdgcn_readfirstlane(tid >> 6);
    const int b = blockIdx.x;
    const int mb = (b & 7) * 16 + (b >> 4), nh = (b >> 3) & 1;
    const int m0 = mb * 64;
    const int g = nh * 8 + wave;
    const int q = lane >> 4, m = lane & 15;
    const __amdgpu_buffer_rsrc_t rs = __builtin_amdgcn_make_buffer_rsrc(ws, 0, WS_BYTES, 0x00020000);

    if (wave < 4) {
        const int combo = (b & 7) + 8 * (b >> 5), pj = (b >> 3) & 3;
        const int pg = combo >> 2, pQ = combo & 3;
        const int aq = wave;
        const int sl = lane >> 4, run = (lane >> 2) & 3, gi = lane & 3;
        const int n0 = 64 * pg + 32 * (pj >> 1) + 8 * run + 4 * (pj & 1);
        const int k0 = 256 * pQ + 64 * sl + 16 * aq + 4 * gi;
        u32x2 v[4];
#pragma unroll
        for (int i = 0; i < 4; ++i) v[i] = *(const GAS u32x2*)(w + (size_t)(k0 + i) * NN + n0);
        u64 ep = 0;
        if (wave == 0) ep = __hip_atomic_load((GAS u64*)(ws + WS_EPOCH), RLX_AGENT);
        if (tid < 16) sy[tid] = 0;
        asm volatile("s_waitcnt lgkmcnt(0)" ::: "memory");
        __builtin_amdgcn_s_barrier();
        asm volatile("" : "+v"(v[0]), "+v"(v[1]), "+v"(v[2]), "+v"(v[3])::"memory");
        LAS unsigned* trL = (LAS unsigned*)(L + L_TR + aq * 1024) + (sl * 16 + 4 * run) * 4 + gi;
        LAS unsigned char* ibL = L + L_IB + aq * 256;
        LAS unsigned char* ibW = ibL + 4 * run * 16 + sl * 4 + gi;
        unsigned rest = 0;
#pragma unroll
        for (int p = 0; p < 2; ++p) {
            const unsigned r0 = v[0][p], r1 = v[1][p], r2 = v[2][p], r3 = v[3][p];
            const unsigned a0 = r0 & 0x7fff7fffu, a1 = r1 & 0x7fff7fffu, a2 = r2 & 0x7fff7fffu, a3 = r3 & 0x7fff7fffu;
            const unsigned h1 = pkmax(a0, a1), l1 = pkmin(a0, a1), h2 = pkmax(a2, a3), l2 = pkmin(a2, a3);
            const unsigned thr0 = pkmax(pkmin(h1, h2), pkmax(l1, l2));
            const unsigned thr = pkmax(thr0, 0x00010001u);
            const unsigned D0 = pklt(a0, thr), D1 = pklt(a1, thr), D2 = pklt(a2, thr), D3 = pklt(a3, thr);
            const unsigned Z = pklt(thr0, 0x00010001u);
            const unsigned e0 = bsel(D0, bsel(D1, bsel(D2, r3 & ~D3, r2), r1), r0);
            const unsigned e1 = bsel(D3, bsel(D2, r1, r2), r3) & ~Z;
            const unsigned ia = D0 & 0x00010001u, ib = ia & D1, ic = ib & D2;
            const unsigned ja = D3 & 0x00040004u, jb = ja & D2;
            const unsigned nib = (ia + ib + ic) + (0x000c000cu - ja - jb);
            const unsigned X1 = ~D1 & ~D0 & ~(D2 & D3), X2 = ~D2 & ~D3 & ~(D0 & D1);
            rest |= ((X1 & 0x00010001u) | (X2 & 0x00020002u)) << (2 * p);
            trL[(2 * p) * 4] = __builtin_amdgcn_perm(e1, e0, 0x05040100u);
            trL[(2 * p + 1) * 4] = __builtin_amdgcn_perm(e1, e0, 0x07060302u);
            ibW[(2 * p) * 16] = (unsigned char)nib;
            ibW[(2 * p + 1) * 16] = (unsigned char)(nib >> 16);
        }
        const int cnt = __builtin_popcount(rest);
        const u64 bal0 = __ballot(cnt & 1);
        int pre = __builtin_amdgcn_mbcnt_hi((unsigned)(bal0 >> 32), __builtin_amdgcn_mbcnt_lo((unsigned)bal0, 0));
        int wsum = __builtin_popcountll(bal0);
        if (__ballot(cnt > 1)) {
#pragma unroll
            for (int bit = 1; bit < 4; ++bit) {
                const u64 bal = __ballot((cnt >> bit) & 1);
                pre += __builtin_amdgcn_mbcnt_hi((unsigned)(bal >> 32), __builtin_amdgcn_mbcnt_lo((unsigned)bal, 0)) << bit;
                wsum += __builtin_popcountll(bal) << bit;
            }
        }
        if (lane == 0) sy[4 + aq] = (unsigned)wsum + 1u;
        if (wave == 0 && lane == 0) {
            sy[8] = (unsigned)ep;
            sy[9] = (unsigned)(ep >> 32);
        }
        asm volatile("s_waitcnt lgkmcnt(0)" ::: "memory");
        {
            const u32x4 d = *(const LAS u32x4*)(L + L_TR + aq * 1024 + lane * 16);
            const u32x4 ib = *(const LAS u32x4*)(ibL + (lane & 15) * 16);
            __builtin_amdgcn_raw_buffer_store_b128(d, rs, WS_WA + (((pg * 16 + 4 * pQ + sl) * 4 + pj) * 64 + aq * 16 + (lane & 15)) * 16, 0, SC1);
            if (lane < 16) {
                unsigned h[4];
#pragma unroll
                for (int s = 0; s < 4; ++s) h[s] = ib[s] | (ib[s] >> 4);
                __builtin_amdgcn_raw_buffer_store_b64(u32x2{__builtin_amdgcn_perm(h[1], h[0], 0x06040200u), __builtin_amdgcn_perm(h[3], h[2], 0x06040200u)}, rs,
                                                      WS_IA + (((pg * 4 + pQ) * 64 + aq * 16 + lane) * 4 + pj) * 8, 0, SC1);
            }
        }
        u32x4 wt;
        for (int sp = 0; sp < SPIN_CAP; ++sp) {
            wt = *(volatile LAS u32x4*)(L + L_SY + 16);
            if (wt[0] && wt[1] && wt[2] && wt[3]) break;
        }
        int base = 0, total = 0;
#pragma unroll
        for (int i = 0; i < 4; ++i) {
            const int t = (int)wt[i] - 1;
            if (i < aq) base += t;
            total += t;
        }
        total = __builtin_amdgcn_readfirstlane(total);
        const int ebase = WS_E + ((pg * 4 + pj) * 4 + pQ) * ECAP * 8;
        int p = base + pre;
        unsigned rr = rest;
        while (rr) {
            const int e = __builtin_ctz(rr);
            rr &= rr - 1;
            const int h = e >> 4, pp = (e >> 1) & 1, t = e & 1;
            const u32x2 vv = t ? v[2] : v[1];
            const unsigned val = ((pp ? vv[1] : vv[0]) >> (h * 16)) & 0xffffu;
            const int k = k0 + 1 + t;
            __builtin_amdgcn_raw_buffer_store_b64(u32x2{(unsigned)((k >> 7) * 16384 + (k & 127) * 2), (val << 16) | (unsigned)(4 * run + 2 * pp + h)}, rs,
                                                  ebase + p * 8, 0, SC1);
            ++p;
        }
        asm volatile("s_waitcnt vmcnt(0)" ::: "memory");
        unsigned old = 0;
        if (lane == 0) old = __hip_atomic_fetch_add((LAS unsigned*)(L + L_SY + 8), 1u, __ATOMIC_RELAXED, __HIP_MEMORY_SCOPE_WORKGROUP);
        old = __builtin_amdgcn_readfirstlane(old);
        if (old == 3) {
            const u64 e = ((u64)sy[9] << 32) | sy[8];
            const u64 fl = ((e + 1ull) << 12) | (u64)total;
            if (lane < NREP) __hip_atomic_store((GAS u64*)(ws + WS_FLAG + ((lane * 16 + pg) * 16 + pj * 4 + pQ) * 8), fl, RLX_AGENT);
        }
    } else {
        __builtin_amdgcn_s_barrier();
        const int lw = wave - 4;
        const int rowl = lw * 16 + q;
#pragma unroll
        for (int c = 0; c < 8; ++c) {
#pragma unroll
            for (int p = 0; p < 4; ++p) {
                const int row = rowl + 4 * p;
                const u16* src = x + (size_t)(m0 + row) * KK + c * 128 + ((m ^ (row & 15)) * 8);
                __builtin_amdgcn_global_load_lds((const GAS unsigned*)src, (LAS unsigned*)(L + c * 16384 + (lw * 4 + p) * 1024), 16, 0, X_AUX);
                if (c * 4 + p >= XFLY) asm volatile("s_waitcnt vmcnt(%0)" ::"i"(XFLY) : "memory");
            }
        }
        asm volatile("s_waitcnt vmcnt(0)" ::: "memory");
        if (lane == 0) __hip_atomic_fetch_add((LAS unsigned*)(L + L_SY), 1u, __ATOMIC_RELAXED, __HIP_MEMORY_SCOPE_WORKGROUP);
    }

    f32x4 acc[4][4];
    {
        const u32x4 b0 = *(const GAS u32x4*)(bias + 64 * g + 8 * q);
        const u32x4 b1 = *(const GAS u32x4*)(bias + 64 * g + 32 + 8 * q);
        f32x4 init[4];
        init[0] = f32x4{__builtin_bit_cast(float, b0[0] << 16), __builtin_bit_cast(float, b0[0] & 0xffff0000u),
                        __builtin_bit_cast(float, b0[1] << 16), __builtin_bit_cast(float, b0[1] & 0xffff0000u)};
        init[1] = f32x4{__builtin_bit_cast(float, b0[2] << 16), __builtin_bit_cast(float, b0[2] & 0xffff0000u),
                        __builtin_bit_cast(float, b0[3] << 16), __builtin_bit_cast(float, b0[3] & 0xffff0000u)};
        init[2] = f32x4{__builtin_bit_cast(float, b1[0] << 16), __builtin_bit_cast(float, b1[0] & 0xffff0000u),
                        __builtin_bit_cast(float, b1[1] << 16), __builtin_bit_cast(float, b1[1] & 0xffff0000u)};
        init[3] = f32x4{__builtin_bit_cast(float, b1[2] << 16), __builtin_bit_cast(float, b1[2] & 0xffff0000u),
                        __builtin_bit_cast(float, b1[3] << 16), __builtin_bit_cast(float, b1[3] & 0xffff0000u)};
#pragma unroll
        for (int j = 0; j < 4; ++j)
#pragma unroll
            for (int i = 0; i < 4; ++i) acc[j][i] = init[j];
    }
    LAS unsigned char* EC = L + L_EC + wave * 2048;
    *(LAS u32x4*)(EC + lane * 16) = u32x4{0, 0, 0, 0};
    *(LAS u32x4*)(EC + 1024 + lane * 16) = u32x4{0, 0, 0, 0};

    u64 tag = 0;
    if (wave == 0) {
        for (int sp = 0; sp < SPIN_CAP; ++sp) {
            if (sy[0] == 4u) break;
            __builtin_amdgcn_s_sleep(1);
        }
        tag = ((((u64)sy[9] << 32) | sy[8]) + 1ull) & ((1ull << 52) - 1ull);
        const int foff = WS_FLAG + ((mb & (NREP - 1)) * 16 + nh * 8) * 128 + lane * 16;
        u32x4 f;
        for (int sp = 0; sp < POLL_CAP; ++sp) {
            f = __builtin_amdgcn_raw_buffer_load_b128(rs, foff, 0, SC1);
            asm volatile("" ::: "memory");
            const u64 f0 = ((u64)f[1] << 32) | f[0], f1 = ((u64)f[3] << 32) | f[2];
            if (__all((f0 >> 12) == tag && (f1 >> 12) == tag)) break;
            __builtin_amdgcn_s_sleep(POLL_SLEEP);
        }
        *(LAS u32x2*)(L + L_CN + lane * 8) = u32x2{f[0] & 0xfffu, f[2] & 0xfffu};
        asm volatile("s_waitcnt lgkmcnt(0)" ::: "memory");
        if (lane == 0) sy[3] = 1u;
    } else {
        for (int sp = 0; sp < SPIN_CAP; ++sp) {
            if (sy[3]) break;
            __builtin_amdgcn_s_sleep(1);
        }
    }
    __builtin_amdgcn_fence(__ATOMIC_ACQUIRE, "wavefront");
    asm volatile("" ::: "memory");

    const int eQ = (lane >> 2) & 3, eu = lane & 3;
    const int eoff = WS_E + ((g * 4 + q) * 4 + eQ) * ECAP * 8 + eu * 32;
    const u32x4 er0 = __builtin_amdgcn_raw_buffer_load_b128(rs, eoff, 0, SC1);
    const u32x4 er1 = __builtin_amdgcn_raw_buffer_load_b128(rs, eoff + 16, 0, SC1);
    const int waoff = WS_WA + g * 65536, iaoff = WS_IA + g * 8192;
    const int rot = (b >> 4) & 3;
    u32x4 A[16][4];
    u32x4 I[4][2];
    auto loadA = [&](int s) {
        if ((s & 3) == 0) {
            I[s >> 2][0] = __builtin_amdgcn_raw_buffer_load_b128(rs, lane * 32, iaoff + (((s >> 2) + rot) & 3) * 2048, SC1);
            I[s >> 2][1] = __builtin_amdgcn_raw_buffer_load_b128(rs, lane * 32 + 16, iaoff + (((s >> 2) + rot) & 3) * 2048, SC1);
        }
#pragma unroll
        for (int j = 0; j < 4; ++j) A[s][j] = __builtin_amdgcn_raw_buffer_load_b128(rs, lane * 16 + j * 1024, waoff + ((((s >> 2) + rot) & 3) * 4 + (s & 3)) * 4096, SC1);
    };
#pragma unroll
    for (int s = 0; s < PF; ++s) loadA(s);
    __builtin_amdgcn_sched_barrier(0);

    const u32x4 cn = *(const volatile LAS u32x4*)(L + L_CN + (wave * 16 + q * 4) * 4);
    const unsigned ctot = cn[0] + cn[1] + cn[2] + cn[3];
    const unsigned cbig = max(max(cn[0], cn[1]), max(cn[2], cn[3]));
    const bool irregular = cbig > 16u;
    {
        const unsigned cme = eQ == 0 ? cn[0] : (eQ == 1 ? cn[1] : (eQ == 2 ? cn[2] : cn[3]));
        const unsigned pre = (eQ > 0 ? cn[0] : 0u) + (eQ > 1 ? cn[1] : 0u) + (eQ > 2 ? cn[2] : 0u);
#pragma unroll
        for (int e = 0; e < 4; ++e) {
            const unsigned idx = (unsigned)(4 * eu + e);
            const unsigned sg = pre + idx;
            if (idx < cme && !irregular) {
                LAS unsigned* dst = (LAS unsigned*)(EC + q * 512 + (sg >> 5) * 256 + (sg & 3) * 32 + ((sg >> 2) & 7) * 4);
                dst[0] = e < 2 ? er0[2 * e] : er1[2 * e - 4];
                dst[32] = e < 2 ? er0[2 * e + 1] : er1[2 * e - 3];
            }
        }
    }
    const bool more = __any(ctot > 32u || irregular);
    asm volatile("s_waitcnt lgkmcnt(0)" ::: "memory");

    const int xb = m * 256 + ((q ^ m) * 16);
    auto loadB = [&](int s, bf16x16 (&B)[4]) {
#pragma unroll
        for (int i = 0; i < 4; ++i) {
            const int off = ((s >> 1) & 1) * 16384 + i * 4096;
            const int qb = (((s >> 2) + rot) & 3) * 32768;
            const u32x4 lo = *(const LAS u32x4*)(L + ((xb ^ ((s & 1) * 128)) + qb + off));
            const u32x4 hi = *(const LAS u32x4*)(L + ((xb ^ ((s & 1) * 128 + 64)) + qb + off));
            const u32x8 t = __builtin_shufflevector(lo, hi, 0, 1, 2, 3, 4, 5, 6, 7);
            B[i] = __builtin_bit_cast(bf16x16, t);
        }
    };
    unsigned ad[8];
    u32x4 af[2];
    unsigned gw[2][8];
    auto setup_tile = [&](int j) {
        const LAS u32x4* t = (const LAS u32x4*)(EC + j * 512 + q * 32);
        const u32x4 a0 = t[0], a1 = t[1], v0 = t[8], v1 = t[9];
        unsigned vv[8];
#pragma unroll
        for (int i = 0; i < 8; ++i) {
            const unsigned a = i < 4 ? a0[i] : a1[i - 4];
            const unsigned v = i < 4 ? v0[i] : v1[i - 4];
            ad[i] = (a ^ (unsigned)(m * 16)) + (unsigned)(m * 256);
            vv[i] = ((u16)v == (u16)m) ? v : 0u;
        }
        af[j & 1] = u32x4{__builtin_amdgcn_perm(vv[1], vv[0], 0x07060302u), __builtin_amdgcn_perm(vv[3], vv[2], 0x07060302u),
                          __builtin_amdgcn_perm(vv[5], vv[4], 0x07060302u), __builtin_amdgcn_perm(vv[7], vv[6], 0x07060302u)};
    };
    auto extra_mfma = [&](int t) {
        u32x4 bw;
#pragma unroll
        for (int p = 0; p < 4; ++p) bw[p] = gw[t & 1][2 * p] | (gw[t & 1][2 * p + 1] << 16);
        acc[t >> 2][t & 3] = __builtin_amdgcn_mfma_f32_16x16x32_bf16(__builtin_bit_cast(bf16x8, af[(t >> 2) & 1]), __builtin_bit_cast(bf16x8, bw), acc[t >> 2][t & 3], 0, 0, 0);
    };
    bf16x16 B[2][4];
    loadB(0, B[0]);
    setup_tile(0);
    __builtin_amdgcn_sched_barrier(0);
    auto step = [&](auto sc) {
        constexpr int s = decltype(sc)::value;
        if constexpr (s + 1 < 16) loadB(s + 1, B[(s + 1) & 1]);
        if constexpr (s + PF < 16) loadA(s + PF);
#pragma unroll
        for (int e = 0; e < 8; ++e) gw[s & 1][e] = *(const LAS u16*)(L + ad[e] + (s & 3) * 4096);
        if constexpr ((s & 3) == 3 && s < 15) setup_tile((s >> 2) + 1);
#pragma unroll
        for (int j = 0; j < 4; ++j) {
            const bf16x8 a = __builtin_bit_cast(bf16x8, A[s][j]);
            const int id = (int)I[s >> 2][j >> 1][(j & 1) * 2 + ((s & 3) >> 1)];
#pragma unroll
            for (int i = 0; i < 4; ++i) acc[j][i] = __builtin_amdgcn_smfmac_f32_16x16x64_bf16(a, B[s & 1][i], acc[j][i], id, 0, s & 1);
        }
        if constexpr (s > 0) extra_mfma(s - 1);
#pragma unroll
        for (int u = 0; u < 8; ++u) {
            __builtin_amdgcn_sched_group_barrier(0x8, 1, 0);
            __builtin_amdgcn_sched_group_barrier(0x100, 2, 0);
            __builtin_amdgcn_sched_group_barrier(0x2, 1, 0);
        }
#pragma unroll
        for (int u = 0; u < 6; ++u) {
            __builtin_amdgcn_sched_group_barrier(0x8, 1, 0);
            __builtin_amdgcn_sched_group_barrier(0x20, 1, 0);
            __builtin_amdgcn_sched_group_barrier(0x100, 1, 0);
            __builtin_amdgcn_sched_group_barrier(0x2, 2, 0);
        }
#pragma unroll
        for (int u = 0; u < 3; ++u) {
            __builtin_amdgcn_sched_group_barrier(0x8, 1, 0);
            __builtin_amdgcn_sched_group_barrier(0x2, 6, 0);
        }
        __builtin_amdgcn_sched_barrier(0);
    };
#define ST(n) step(IC<n>{});
    ST(0) ST(1) ST(2) ST(3) ST(4) ST(5) ST(6) ST(7) ST(8) ST(9) ST(10) ST(11) ST(12) ST(13) ST(14) ST(15)
#undef ST
    extra_mfma(15);
    int tz = threadIdx.x;
    asm volatile("" : "+v"(tz));
    const int q2 = (tz >> 4) & 3, m2 = tz & 15;
#define q q2
#define m m2
    if (more) {
#pragma unroll
        for (int j = 0; j < 4; ++j) {
            const u32x4 c4 = *(const volatile LAS u32x4*)(L + L_CN + (wave * 16 + j * 4) * 4);
            const int ct = __builtin_amdgcn_readfirstlane((int)(c4[0] + c4[1] + c4[2] + c4[3]));
            const int cb = __builtin_amdgcn_readfirstlane((int)max(max(c4[0], c4[1]), max(c4[2], c4[3])));
            if (cb > 16) {
                const int cq = (int)*(const volatile LAS unsigned*)(L + L_CN + (wave * 16 + j * 4 + q) * 4);
                const int lb = WS_E + ((g * 4 + j) * 4 + q) * ECAP * 8;
                for (int r = 0; r * 8 < cb; ++r) {
                    u32x4 e[4];
#pragma unroll
                    for (int u = 0; u < 4; ++u) e[u] = __builtin_amdgcn_raw_buffer_load_b128(rs, lb + min(r * 64 + u * 16, ECAP * 8 - 16), 0, SC1);
                    u32x4 a0, a1, v0, v1;
#pragma unroll
                    for (int i = 0; i < 4; ++i) {
                        const bool ok0 = r * 8 + i < cq, ok1 = r * 8 + 4 + i < cq;
                        a0[i] = ok0 ? e[i >> 1][(i & 1) * 2] : 0u;
                        v0[i] = ok0 ? e[i >> 1][(i & 1) * 2 + 1] : 0u;
                        a1[i] = ok1 ? e[2 + (i >> 1)][(i & 1) * 2] : 0u;
                        v1[i] = ok1 ? e[2 + (i >> 1)][(i & 1) * 2 + 1] : 0u;
                    }
                    extras_round(L, a0, a1, v0, v1, m, acc[j]);
                }
            } else if (ct > 32) {
                const LAS u32x4* t = (const LAS u32x4*)(EC + j * 512 + 256 + q * 32);
                extras_round(L, t[0], t[1], t[8], t[9], m, acc[j]);
            }
        }
    }

    const __amdgpu_buffer_rsrc_t ro = __builtin_amdgcn_make_buffer_rsrc(out, 0, MM * NN * 2, 0x00020000);
#pragma unroll
    for (int i = 0; i < 4; ++i) {
#pragma unroll
        for (int p = 0; p < 2; ++p) {
            u32x4 r;
            r[0] = pack_bf16(acc[2 * p][i][0], acc[2 * p][i][1]);
            r[1] = pack_bf16(acc[2 * p][i][2], acc[2 * p][i][3]);
            r[2] = pack_bf16(acc[2 * p + 1][i][0], acc[2 * p + 1][i][1]);
            r[3] = pack_bf16(acc[2 * p + 1][i][2], acc[2 * p + 1][i][3]);
            __builtin_amdgcn_raw_buffer_store_b128(r, ro, ((m0 + i * 16 + m) * NN + 64 * g + 8 * q + 32 * p) * 2, 0, ST_AUX);
        }
    }
#undef q
#undef m

    if (b == 0 && wave == 0) {
        GAS u64* fp = (GAS u64*)(ws + WS_FLAG) + lane;
        for (int sp = 0; sp < POLL_CAP; ++sp) {
            bool ok = true;
#pragma unroll
            for (int u = 0; u < 4; ++u) ok &= (__hip_atomic_load(fp + 64 * u, RLX_AGENT) >> 12) == tag;
            if (__all(ok)) break;
            __builtin_amdgcn_s_sleep(2);
        }
        if (lane == 0) __hip_atomic_store((GAS u64*)(ws + WS_EPOCH), (((u64)sy[9] << 32) | sy[8]) + 1ull, RLX_AGENT);
    }
}

extern "C" void kernel_launch(void* const* d_in, const int* in_sizes, int n_in, void* d_out, int out_size, void* d_ws,
                              size_t ws_size, hipStream_t stream) {
    const u16* x = (const u16*)d_in[0];
    const u16* w = (const u16*)d_in[1];
    const u16* b = (const u16*)d_in[2];
    (void)hipFuncSetAttribute((const void*)fused_kernel, hipFuncAttributeMaxDynamicSharedMemorySize, LDS_BYTES);
    fused_kernel<<<256, 512, LDS_BYTES, stream>>>(x, w, b, (char*)d_ws, (u16*)d_out);
}
```

```cpp
#include <hip/hip_runtime.h>
#include <hip/hip_bf16.h>

typedef unsigned short u16;
typedef unsigned long long u64;
typedef __attribute__((ext_vector_type(8))) __bf16 bf16x8;
typedef __attribute__((ext_vector_type(16))) __bf16 bf16x16;
typedef __attribute__((ext_vector_type(4))) float f32x4;
typedef __attribute__((ext_vector_type(2))) unsigned u32x2;
typedef __attribute__((ext_vector_type(4))) unsigned u32x4;
typedef __attribute__((ext_vector_type(8))) unsigned u32x8;
#define LAS __attribute__((address_space(3)))
#define GAS __attribute__((address_space(1)))
#define RLX_AGENT __ATOMIC_RELAXED, __HIP_MEMORY_SCOPE_AGENT
#define SC1 16
#define X_AUX 2
#define ST_AUX 18
#define PF 2
#define XCH 2
#define POLL_SLEEP 4

constexpr int MM = 8192, KK = 1024, NN = 1024;
constexpr unsigned WS_EPOCH = 0;
constexpr unsigned WS_FLAG = 4096;
constexpr unsigned WS_E16 = 65536;
constexpr unsigned WS_WA = 1u << 20;
constexpr unsigned WS_IA = 2u << 20;
constexpr unsigned WS_E = 3u << 20;
constexpr unsigned WS_BYTES = 7u << 20;
constexpr int ECAP = 2048;
constexpr int NREP = 16;
constexpr int L_X = 0, L_TR = 131072, L_IB = L_TR + 4096, L_SY = L_IB + 1024, L_CN = L_SY + 64, L_EC = L_CN + 512,
              LDS_BYTES = L_EC + 8 * 2048;
constexpr int SPIN_CAP = 1 << 21;
constexpr int POLL_CAP = 1 << 17;

template <int V> struct IC { static constexpr int value = V; };
__device__ __forceinline__ unsigned pack_bf16(float lo, float hi) {
    typedef __attribute__((ext_vector_type(2))) __bf16 bf16x2;
    bf16x2 r;
    r[0] = (__bf16)lo;
    r[1] = (__bf16)hi;
    return __builtin_bit_cast(unsigned, r);
}
typedef __attribute__((ext_vector_type(2))) unsigned short us2;
typedef __attribute__((ext_vector_type(2))) short ss2;
__device__ __forceinline__ unsigned pkmax(unsigned a, unsigned b) {
    return __builtin_bit_cast(unsigned, __builtin_elementwise_max(__builtin_bit_cast(us2, a), __builtin_bit_cast(us2, b)));
}
__device__ __forceinline__ unsigned pkmin(unsigned a, unsigned b) {
    return __builtin_bit_cast(unsigned, __builtin_elementwise_min(__builtin_bit_cast(us2, a), __builtin_bit_cast(us2, b)));
}
__device__ __forceinline__ unsigned pklt(unsigned a, unsigned b) {
    const ss2 d = __builtin_bit_cast(ss2, a) - __builtin_bit_cast(ss2, b);
    unsigned r = __builtin_bit_cast(unsigned, (ss2)(d >> (short)15));
    asm("" : "+v"(r));
    return r;
}
__device__ __forceinline__ unsigned bsel(unsigned m, unsigned a, unsigned b) { return (m & a) | (~m & b); }

__device__ __forceinline__ void extras_round(LAS unsigned char* L, const u32x4 ea0, const u32x4 ea1, const u32x4 ev0, const u32x4 ev1, int ne, int m,
                                             f32x4 (&acc)[4]) {
    u32x4 aw, bw[4];
#pragma unroll
    for (int p = 0; p < 4; ++p) {
        if (2 * p < ne) {
            const unsigned a0 = p < 2 ? ea0[2 * p] : ea1[2 * p - 4], a1 = p < 2 ? ea0[2 * p + 1] : ea1[2 * p - 3];
            const unsigned v0 = p < 2 ? ev0[2 * p] : ev1[2 * p - 4], v1 = p < 2 ? ev0[2 * p + 1] : ev1[2 * p - 3];
            const unsigned d0 = (a0 ^ (unsigned)(m * 16)) + (unsigned)(m * 256), d1 = (a1 ^ (unsigned)(m * 16)) + (unsigned)(m * 256);
            aw[p] = __builtin_amdgcn_perm(((u16)v1 == (u16)m) ? v1 : 0u, ((u16)v0 == (u16)m) ? v0 : 0u, 0x07060302u);
#pragma unroll
            for (int i = 0; i < 4; ++i) {
                const unsigned lo = *(const LAS u16*)(L + d0 + i * 4096);
                const unsigned hi = *(const LAS u16*)(L + d1 + i * 4096);
                bw[i][p] = lo | (hi << 16);
            }
        } else {
            aw[p] = 0u;
#pragma unroll
            for (int i = 0; i < 4; ++i) bw[i][p] = 0u;
        }
    }
#pragma unroll
    for (int i = 0; i < 4; ++i)
        acc[i] = __builtin_amdgcn_mfma_f32_16x16x32_bf16(__builtin_bit_cast(bf16x8, aw), __builtin_bit_cast(bf16x8, bw[i]), acc[i], 0, 0, 0);
}

__global__ __launch_bounds__(512, 2) void fused_kernel(const u16* __restrict__ x, const u16* __restrict__ w,
                                                       const u16* __restrict__ bias, char* ws, u16* __restrict__ out) {
    extern __shared__ __attribute__((aligned(16))) unsigned char lds[];
    LAS unsigned char* L = (LAS unsigned char*)lds;
    volatile LAS unsigned* sy = (volatile LAS unsigned*)(L + L_SY);
    asm volatile("" ::"s"(x), "s"(w), "s"(bias), "s"(ws), "s"(out));
    const int tid = threadIdx.x, lane = tid & 63;
    const int wave = __builtin_amdgcn_readfirstlane(tid >> 6);
    const int b = blockIdx.x;
    const int mb = (b & 7) * 16 + (b >> 4), nh = (b >> 3) & 1;
    const int m0 = mb * 64;
    const int g = nh * 8 + wave;
    const int q = lane >> 4, m = lane & 15;
    const __amdgpu_buffer_rsrc_t rs = __builtin_amdgcn_make_buffer_rsrc(ws, 0, WS_BYTES, 0x00020000);
    const int rot = (b >> 4) & 3;

    if (wave < 4) {
        const int combo = (b & 7) + 8 * (b >> 5), pj = (b >> 3) & 3;
        const int pg = combo >> 2, pQ = combo & 3;
        const int aq = wave;
        const int sl = lane >> 4, run = (lane >> 2) & 3, gi = lane & 3;
        const int n0 = 64 * pg + 32 * (pj >> 1) + 8 * run + 4 * (pj & 1);
        const int k0 = 256 * pQ + 64 * sl + 16 * aq + 4 * gi;
        u32x2 v[4];
#pragma unroll
        for (int i = 0; i < 4; ++i) v[i] = *(const GAS u32x2*)(w + (size_t)(k0 + i) * NN + n0);
        u64 ep = 0;
        if (wave == 0) ep = __hip_atomic_load((GAS u64*)(ws + WS_EPOCH), RLX_AGENT);
        if (tid < 16) sy[tid] = 0;
        asm volatile("s_waitcnt lgkmcnt(0)" ::: "memory");
        __builtin_amdgcn_s_barrier();
        asm volatile("" : "+v"(v[0]), "+v"(v[1]), "+v"(v[2]), "+v"(v[3])::"memory");
        LAS unsigned* trL = (LAS unsigned*)(L + L_TR + aq * 1024) + (sl * 16 + 4 * run) * 4 + gi;
        LAS unsigned char* ibL = L + L_IB + aq * 256;
        LAS unsigned char* ibW = ibL + 4 * run * 16 + sl * 4 + gi;
        unsigned rest = 0;
#pragma unroll
        for (int p = 0; p < 2; ++p) {
            const unsigned r0 = v[0][p], r1 = v[1][p], r2 = v[2][p], r3 = v[3][p];
            const unsigned a0 = r0 & 0x7fff7fffu, a1 = r1 & 0x7fff7fffu, a2 = r2 & 0x7fff7fffu, a3 = r3 & 0x7fff7fffu;
            const unsigned h1 = pkmax(a0, a1), l1 = pkmin(a0, a1), h2 = pkmax(a2, a3), l2 = pkmin(a2, a3);
            const unsigned thr0 = pkmax(pkmin(h1, h2), pkmax(l1, l2));
            const unsigned thr = pkmax(thr0, 0x00010001u);
            const unsigned D0 = pklt(a0, thr), D1 = pklt(a1, thr), D2 = pklt(a2, thr), D3 = pklt(a3, thr);
            const unsigned Z = pklt(thr0, 0x00010001u);
            const unsigned e0 = bsel(D0, bsel(D1, bsel(D2, r3 & ~D3, r2), r1), r0);
            const unsigned e1 = bsel(D3, bsel(D2, r1, r2), r3) & ~Z;
            const unsigned ia = D0 & 0x00010001u, ib = ia & D1, ic = ib & D2;
            const unsigned ja = D3 & 0x00040004u, jb = ja & D2;
            const unsigned nib = (ia + ib + ic) + (0x000c000cu - ja - jb);
            const unsigned X1 = ~D1 & ~D0 & ~(D2 & D3), X2 = ~D2 & ~D3 & ~(D0 & D1);
            rest |= ((X1 & 0x00010001u) | (X2 & 0x00020002u)) << (2 * p);
            trL[(2 * p) * 4] = __builtin_amdgcn_perm(e1, e0, 0x05040100u);
            trL[(2 * p + 1) * 4] = __builtin_amdgcn_perm(e1, e0, 0x07060302u);
            ibW[(2 * p) * 16] = (unsigned char)nib;
            ibW[(2 * p + 1) * 16] = (unsigned char)(nib >> 16);
        }
        const int cnt = __builtin_popcount(rest);
        const u64 bal0 = __ballot(cnt & 1);
        int pre = __builtin_amdgcn_mbcnt_hi((unsigned)(bal0 >> 32), __builtin_amdgcn_mbcnt_lo((unsigned)bal0, 0));
        int wsum = __builtin_popcountll(bal0);
        if (__ballot(cnt > 1)) {
#pragma unroll
            for (int bit = 1; bit < 4; ++bit) {
                const u64 bal = __ballot((cnt >> bit) & 1);
                pre += __builtin_amdgcn_mbcnt_hi((unsigned)(bal >> 32), __builtin_amdgcn_mbcnt_lo((unsigned)bal, 0)) << bit;
                wsum += __builtin_popcountll(bal) << bit;
            }
        }
        if (lane == 0) sy[4 + aq] = (unsigned)wsum + 1u;
        if (wave == 0 && lane == 0) {
            sy[8] = (unsigned)ep;
            sy[9] = (unsigned)(ep >> 32);
        }
        asm volatile("s_waitcnt lgkmcnt(0)" ::: "memory");
        {
            const u32x4 d = *(const LAS u32x4*)(L + L_TR + aq * 1024 + lane * 16);
            const u32x4 ib = *(const LAS u32x4*)(ibL + (lane & 15) * 16);
            __builtin_amdgcn_raw_buffer_store_b128(d, rs, WS_WA + (((pg * 16 + 4 * pQ + sl) * 4 + pj) * 64 + aq * 16 + (lane & 15)) * 16, 0, SC1);
            if (lane < 16) {
                unsigned h[4];
#pragma unroll
                for (int s = 0; s < 4; ++s) h[s] = ib[s] | (ib[s] >> 4);
                __builtin_amdgcn_raw_buffer_store_b64(u32x2{__builtin_amdgcn_perm(h[1], h[0], 0x06040200u), __builtin_amdgcn_perm(h[3], h[2], 0x06040200u)}, rs,
                                                      WS_IA + ((((pg * 4 + pQ) * 2 + (pj >> 1)) * 64 + aq * 16 + lane) * 2 + (pj & 1)) * 8, 0, SC1);
            }
        }
        u32x4 wt;
        for (int sp = 0; sp < SPIN_CAP; ++sp) {
            wt = *(volatile LAS u32x4*)(L + L_SY + 16);
            if (wt[0] && wt[1] && wt[2] && wt[3]) break;
        }
        int base = 0, total = 0;
#pragma unroll
        for (int i = 0; i < 4; ++i) {
            const int t = (int)wt[i] - 1;
            if (i < aq) base += t;
            total += t;
        }
        total = __builtin_amdgcn_readfirstlane(total);
        const int ebase = WS_E + ((pg * 4 + pj) * 4 + pQ) * ECAP * 8;
        int p = base + pre;
        unsigned rr = rest;
        while (rr) {
            const int e = __builtin_ctz(rr);
            rr &= rr - 1;
            const int h = e >> 4, pp = (e >> 1) & 1, t = e & 1;
            const u32x2 vv = t ? v[2] : v[1];
            const unsigned val = ((pp ? vv[1] : vv[0]) >> (h * 16)) & 0xffffu;
            const int k = k0 + 1 + t;
            const u32x2 ent = u32x2{(unsigned)((k >> 7) * 16384 + (k & 127) * 2), (val << 16) | (unsigned)(4 * run + 2 * pp + h)};
            if (p < 16) __builtin_amdgcn_raw_buffer_store_b64(ent, rs, WS_E16 + (((pg * 4 + pj) * 4 + pQ) * 16 + p) * 8, 0, SC1);
            if (total > 16) __builtin_amdgcn_raw_buffer_store_b64(ent, rs, ebase + p * 8, 0, SC1);
            ++p;
        }
        asm volatile("s_waitcnt vmcnt(0)" ::: "memory");
        unsigned old = 0;
        if (lane == 0) old = __hip_atomic_fetch_add((LAS unsigned*)(L + L_SY + 8), 1u, __ATOMIC_RELAXED, __HIP_MEMORY_SCOPE_WORKGROUP);
        old = __builtin_amdgcn_readfirstlane(old);
        if (old == 3) {
            const u64 e = ((u64)sy[9] << 32) | sy[8];
            const u64 fl = ((e + 1ull) << 12) | (u64)total;
            if (lane < NREP) __hip_atomic_store((GAS u64*)(ws + WS_FLAG + ((lane * 16 + pg) * 16 + pj * 4 + pQ) * 8), fl, RLX_AGENT);
        }
    } else {
        __builtin_amdgcn_s_barrier();
        const int lw = wave - 4;
        const int rowl = lw * 16 + q;
#pragma unroll
        for (int c = 0; c < 8; ++c) {
            const int cc = (c + 2 * rot) & 7;
#pragma unroll
            for (int p = 0; p < 4; ++p) {
                const int row = rowl + 4 * p;
                const u16* src = x + (size_t)(m0 + row) * KK + cc * 128 + ((m ^ (row & 15)) * 8);
                __builtin_amdgcn_global_load_lds((const GAS unsigned*)src, (LAS unsigned*)(L + cc * 16384 + (lw * 4 + p) * 1024), 16, 0, X_AUX);
            }
            if (c >= XCH) {
                asm volatile("s_waitcnt vmcnt(%0)" ::"i"(4 * XCH) : "memory");
                if (lane == 0) sy[12 + lw] = c - XCH + 1;
            }
        }
#pragma unroll
        for (int c = 8 - XCH; c < 8; ++c) {
            asm volatile("s_waitcnt vmcnt(%0)" ::"i"(4 * (7 - c)) : "memory");
            if (lane == 0) sy[12 + lw] = c + 1;
        }
    }

    f32x4 acc[4][4];
    {
        const u32x4 b0 = *(const GAS u32x4*)(bias + 64 * g + 8 * q);
        const u32x4 b1 = *(const GAS u32x4*)(bias + 64 * g + 32 + 8 * q);
        f32x4 init[4];
        init[0] = f32x4{__builtin_bit_cast(float, b0[0] << 16), __builtin_bit_cast(float, b0[0] & 0xffff0000u),
                        __builtin_bit_cast(float, b0[1] << 16), __builtin_bit_cast(float, b0[1] & 0xffff0000u)};
        init[1] = f32x4{__builtin_bit_cast(float, b0[2] << 16), __builtin_bit_cast(float, b0[2] & 0xffff0000u),
                        __builtin_bit_cast(float, b0[3] << 16), __builtin_bit_cast(float, b0[3] & 0xffff0000u)};
        init[2] = f32x4{__builtin_bit_cast(float, b1[0] << 16), __builtin_bit_cast(float, b1[0] & 0xffff0000u),
                        __builtin_bit_cast(float, b1[1] << 16), __builtin_bit_cast(float, b1[1] & 0xffff0000u)};
        init[3] = f32x4{__builtin_bit_cast(float, b1[2] << 16), __builtin_bit_cast(float, b1[2] & 0xffff0000u),
                        __builtin_bit_cast(float, b1[3] << 16), __builtin_bit_cast(float, b1[3] & 0xffff0000u)};
#pragma unroll
        for (int j = 0; j < 4; ++j)
#pragma unroll
            for (int i = 0; i < 4; ++i) acc[j][i] = init[j];
    }
    LAS unsigned char* EC = L + L_EC + wave * 2048;
    *(LAS u32x4*)(EC + lane * 16) = u32x4{0, 0, 0, 0};
    *(LAS u32x4*)(EC + 1024 + lane * 16) = u32x4{0, 0, 0, 0};

    if (wave == 0) {
        for (int sp = 0; sp < SPIN_CAP; ++sp) {
            if (sy[2] == 4u) break;
            __builtin_amdgcn_s_sleep(1);
        }
        for (int sp = 0; sp < SPIN_CAP; ++sp) {
            const u32x4 f = *(volatile LAS u32x4*)(L + L_SY + 48);
            if (__builtin_amdgcn_readfirstlane((int)min(min(f[0], f[1]), min(f[2], f[3]))) == 8) break;
            __builtin_amdgcn_s_sleep(1);
        }

        const u64 tag = ((((u64)sy[9] << 32) | sy[8]) + 1ull) & ((1ull << 52) - 1ull);
        const int foff = WS_FLAG + ((mb & (NREP - 1)) * 16 + nh * 8) * 128 + lane * 16;
        for (int sp = 0; sp < POLL_CAP; ++sp) {
            const u32x4 f = __builtin_amdgcn_raw_buffer_load_b128(rs, foff, 0, SC1);
            asm volatile("" ::: "memory");
            const u64 f0 = ((u64)f[1] << 32) | f[0], f1 = ((u64)f[3] << 32) | f[2];
            const bool ok = (f0 >> 12) == tag && (f1 >> 12) == tag;
            if (ok) *(LAS u32x2*)(L + L_CN + lane * 8) = u32x2{f[0] & 0xfffu, f[2] & 0xfffu};
            const u64 bad = ~__ballot(ok);
            unsigned rdy = 0;
#pragma unroll
            for (int u = 0; u < 8; ++u) rdy |= (((bad >> (8 * u)) & 0xffull) == 0ull ? 1u : 0u) << u;
            asm volatile("s_waitcnt lgkmcnt(0)" ::: "memory");
            if (lane == 0) sy[3] = rdy;
            if (rdy == 0xffu) break;
            __builtin_amdgcn_s_sleep(POLL_SLEEP);
        }
    } else {
        for (int sp = 0; sp < SPIN_CAP; ++sp) {
            if ((sy[3] >> wave) & 1u) break;
            __builtin_amdgcn_s_sleep(1);
        }
    }
    __builtin_amdgcn_fence(__ATOMIC_ACQUIRE, "wavefront");
    asm volatile("" ::: "memory");

    const int eQ = (lane >> 2) & 3, eu = lane & 3;
    const int eoff = WS_E16 + g * 2048 + lane * 32;
    const u32x4 er0 = __builtin_amdgcn_raw_buffer_load_b128(rs, eoff, 0, SC1);
    const u32x4 er1 = __builtin_amdgcn_raw_buffer_load_b128(rs, eoff + 16, 0, SC1);
    const int waoff = WS_WA + g * 65536, iaoff = WS_IA + g * 8192;
    u32x4 A[16][4];
    u32x4 I[4][2];
    auto loadA = [&](int s) {
        if ((s & 3) == 0) {
            I[s >> 2][0] = __builtin_amdgcn_raw_buffer_load_b128(rs, lane * 16, iaoff + (((s >> 2) + rot) & 3) * 2048, SC1);
            I[s >> 2][1] = __builtin_amdgcn_raw_buffer_load_b128(rs, lane * 16 + 1024, iaoff + (((s >> 2) + rot) & 3) * 2048, SC1);
        }
#pragma unroll
        for (int j = 0; j < 4; ++j) A[s][j] = __builtin_amdgcn_raw_buffer_load_b128(rs, lane * 16 + j * 1024, waoff + ((((s >> 2) + rot) & 3) * 4 + (s & 3)) * 4096, SC1);
    };
#pragma unroll
    for (int s = 0; s < PF; ++s) loadA(s);
    __builtin_amdgcn_sched_barrier(0);

    const u32x4 cn = *(const volatile LAS u32x4*)(L + L_CN + (wave * 16 + q * 4) * 4);
    const unsigned ctot = cn[0] + cn[1] + cn[2] + cn[3];
    const unsigned cbig = max(max(cn[0], cn[1]), max(cn[2], cn[3]));
    const bool irregular = cbig > 16u;
    {
        const unsigned cme = eQ == 0 ? cn[0] : (eQ == 1 ? cn[1] : (eQ == 2 ? cn[2] : cn[3]));
        const unsigned pre = (eQ > 0 ? cn[0] : 0u) + (eQ > 1 ? cn[1] : 0u) + (eQ > 2 ? cn[2] : 0u);
#pragma unroll
        for (int e = 0; e < 4; ++e) {
            const unsigned idx = (unsigned)(4 * eu + e);
            const unsigned sg = pre + idx;
            if (idx < cme && !irregular) {
                LAS unsigned* dst = (LAS unsigned*)(EC + q * 512 + (sg >> 5) * 256 + (sg & 3) * 32 + ((sg >> 2) & 7) * 4);
                dst[0] = e < 2 ? er0[2 * e] : er1[2 * e - 4];
                dst[32] = e < 2 ? er0[2 * e + 1] : er1[2 * e - 3];
            }
        }
    }
    const bool more = __any(ctot > 32u || irregular);
    asm volatile("s_waitcnt lgkmcnt(0)" ::: "memory");

    unsigned ad[8];
    u32x4 af[2];
    unsigned gw[2][8];
    auto setup_tile = [&](int j) {
        const LAS u32x4* t = (const LAS u32x4*)(EC + j * 512 + q * 32);
        const u32x4 a0 = t[0], a1 = t[1], v0 = t[8], v1 = t[9];
        unsigned vv[8];
#pragma unroll
        for (int i = 0; i < 8; ++i) {
            const unsigned a = i < 4 ? a0[i] : a1[i - 4];
            const unsigned v = i < 4 ? v0[i] : v1[i - 4];
            ad[i] = (a ^ (unsigned)(m * 16)) + (unsigned)(m * 256);
            vv[i] = ((u16)v == (u16)m) ? v : 0u;
        }
        af[j & 1] = u32x4{__builtin_amdgcn_perm(vv[1], vv[0], 0x07060302u), __builtin_amdgcn_perm(vv[3], vv[2], 0x07060302u),
                          __builtin_amdgcn_perm(vv[5], vv[4], 0x07060302u), __builtin_amdgcn_perm(vv[7], vv[6], 0x07060302u)};
    };
    setup_tile(0);
    for (int sp = 0; sp < SPIN_CAP; ++sp) {
        const u32x4 f = *(volatile LAS u32x4*)(L + L_SY + 48);
        if (__builtin_amdgcn_readfirstlane((int)min(min(f[0], f[1]), min(f[2], f[3]))) == 8) break;
        __builtin_amdgcn_s_sleep(1);
    }
    asm volatile("" ::: "memory");
    const int xb = m * 256 + ((q ^ m) * 16);
    auto loadB = [&](int s, bf16x16 (&B)[4]) {
#pragma unroll
        for (int i = 0; i < 4; ++i) {
            const int off = ((s >> 1) & 1) * 16384 + i * 4096;
            const int qb = (((s >> 2) + rot) & 3) * 32768;
            const u32x4 lo = *(const LAS u32x4*)(L + ((xb ^ ((s & 1) * 128)) + qb + off));
            const u32x4 hi = *(const LAS u32x4*)(L + ((xb ^ ((s & 1) * 128 + 64)) + qb + off));
            const u32x8 t = __builtin_shufflevector(lo, hi, 0, 1, 2, 3, 4, 5, 6, 7);
            B[i] = __builtin_bit_cast(bf16x16, t);
        }
    };
    auto extra_mfma = [&](int t) {
        u32x4 bw;
#pragma unroll
        for (int p = 0; p < 4; ++p) bw[p] = gw[t & 1][2 * p] | (gw[t & 1][2 * p + 1] << 16);
        acc[t >> 2][t & 3] = __builtin_amdgcn_mfma_f32_16x16x32_bf16(__builtin_bit_cast(bf16x8, af[(t >> 2) & 1]), __builtin_bit_cast(bf16x8, bw), acc[t >> 2][t & 3], 0, 0, 0);
    };
    bf16x16 B[2][4];
    loadB(0, B[0]);
    __builtin_amdgcn_sched_barrier(0);
    auto step = [&](auto sc) {
        constexpr int s = decltype(sc)::value;
        if constexpr (s + PF < 16) loadA(s + PF);
        if constexpr (s + 1 < 16) loadB(s + 1, B[(s + 1) & 1]);
#pragma unroll
        for (int e = 0; e < 8; ++e) gw[s & 1][e] = *(const LAS u16*)(L + ad[e] + (s & 3) * 4096);
        if constexpr ((s & 3) == 3 && s < 15) setup_tile((s >> 2) + 1);
#pragma unroll
        for (int j = 0; j < 4; ++j) {
            const bf16x8 a = __builtin_bit_cast(bf16x8, A[s][j]);
            const int id = (int)I[s >> 2][j >> 1][(j & 1) * 2 + ((s & 3) >> 1)];
#pragma unroll
            for (int i = 0; i < 4; ++i) acc[j][i] = __builtin_amdgcn_smfmac_f32_16x16x64_bf16(a, B[s & 1][i], acc[j][i], id, 0, s & 1);
        }
        if constexpr (s > 0) extra_mfma(s - 1);
        __builtin_amdgcn_sched_barrier(0);
    };
#define ST(n) step(IC<n>{});
    ST(0) ST(1) ST(2) ST(3) ST(4) ST(5) ST(6) ST(7) ST(8) ST(9) ST(10) ST(11) ST(12) ST(13) ST(14) ST(15)
#undef ST
    extra_mfma(15);
    int tz = threadIdx.x;
    asm volatile("" : "+v"(tz));
    const int q2 = (tz >> 4) & 3, m2 = tz & 15;
#define q q2
#define m m2
    if (more) {
#pragma unroll
        for (int j = 0; j < 4; ++j) {
            const u32x4 c4 = *(const volatile LAS u32x4*)(L + L_CN + (wave * 16 + j * 4) * 4);
            const int ct = __builtin_amdgcn_readfirstlane((int)(c4[0] + c4[1] + c4[2] + c4[3]));
            const int cb = __builtin_amdgcn_readfirstlane((int)max(max(c4[0], c4[1]), max(c4[2], c4[3])));
            if (cb > 16) {
                const int cq = (int)*(const volatile LAS unsigned*)(L + L_CN + (wave * 16 + j * 4 + q) * 4);
                const int lb = cq > 16 ? WS_E + ((g * 4 + j) * 4 + q) * ECAP * 8 : WS_E16 + ((g * 4 + j) * 4 + q) * 128;
                const int lmax = (cq > 16 ? ECAP * 8 : 128) - 16;
                for (int r = 0; r * 8 < cb; ++r) {
                    u32x4 e[4];
#pragma unroll
                    for (int u = 0; u < 4; ++u) e[u] = __builtin_amdgcn_raw_buffer_load_b128(rs, lb + min(r * 64 + u * 16, lmax), 0, SC1);
                    u32x4 a0, a1, v0, v1;
#pragma unroll
                    for (int i = 0; i < 4; ++i) {
                        const bool ok0 = r * 8 + i < cq, ok1 = r * 8 + 4 + i < cq;
                        a0[i] = ok0 ? e[i >> 1][(i & 1) * 2] : 0u;
                        v0[i] = ok0 ? e[i >> 1][(i & 1) * 2 + 1] : 0u;
                        a1[i] = ok1 ? e[2 + (i >> 1)][(i & 1) * 2] : 0u;
                        v1[i] = ok1 ? e[2 + (i >> 1)][(i & 1) * 2 + 1] : 0u;
                    }
                    extras_round(L, a0, a1, v0, v1, cb - r * 8, m, acc[j]);
                }
            } else if (ct > 32) {
                const LAS u32x4* t = (const LAS u32x4*)(EC + j * 512 + 256 + q * 32);
                extras_round(L, t[0], t[1], t[8], t[9], (ct - 32 + 3) >> 2, m, acc[j]);
            }
        }
    }

    const __amdgpu_buffer_rsrc_t ro = __builtin_amdgcn_make_buffer_rsrc(out, 0, MM * NN * 2, 0x00020000);
    const int ooff = ((m0 + (m & 7)) * NN + 64 * g + 32 * (m >> 3) + 8 * q) * 2;
#pragma unroll
    for (int i = 0; i < 4; ++i) {
        u32x4 P0, P1, SA, SB;
        P0[0] = pack_bf16(acc[0][i][0], acc[0][i][1]);
        P0[1] = pack_bf16(acc[0][i][2], acc[0][i][3]);
        P0[2] = pack_bf16(acc[1][i][0], acc[1][i][1]);
        P0[3] = pack_bf16(acc[1][i][2], acc[1][i][3]);
        P1[0] = pack_bf16(acc[2][i][0], acc[2][i][1]);
        P1[1] = pack_bf16(acc[2][i][2], acc[2][i][3]);
        P1[2] = pack_bf16(acc[3][i][0], acc[3][i][1]);
        P1[3] = pack_bf16(acc[3][i][2], acc[3][i][3]);
#pragma unroll
        for (int d = 0; d < 4; ++d) {
            SA[d] = (unsigned)__builtin_amdgcn_update_dpp((int)P0[d], (int)P1[d], 0x128, 0xf, 0xc, false);
            SB[d] = (unsigned)__builtin_amdgcn_update_dpp((int)P1[d], (int)P0[d], 0x128, 0xf, 0x3, false);
        }
        __builtin_amdgcn_raw_buffer_store_b128(SA, ro, ooff, i * 16 * NN * 2, ST_AUX);
        __builtin_amdgcn_raw_buffer_store_b128(SB, ro, ooff, (i * 16 + 8) * NN * 2, ST_AUX);
    }
#undef q
#undef m

    if (b == 0 && wave == 0) {
        const u64 tag = ((((u64)sy[9] << 32) | sy[8]) + 1ull) & ((1ull << 52) - 1ull);
        GAS u64* fp = (GAS u64*)(ws + WS_FLAG) + (tz & 63);
        for (int sp = 0; sp < POLL_CAP; ++sp) {
            bool ok = true;
#pragma unroll
            for (int u = 0; u < 4; ++u) ok &= (__hip_atomic_load(fp + 64 * u, RLX_AGENT) >> 12) == tag;
            if (__all(ok)) break;
            __builtin_amdgcn_s_sleep(2);
        }
        if ((tz & 63) == 0) __hip_atomic_store((GAS u64*)(ws + WS_EPOCH), (((u64)sy[9] << 32) | sy[8]) + 1ull, RLX_AGENT);
    }
}

extern "C" void kernel_launch(void* const* d_in, const int* in_sizes, int n_in, void* d_out, int out_size, void* d_ws,
                              size_t ws_size, hipStream_t stream) {
    const u16* x = (const u16*)d_in[0];
    const u16* w = (const u16*)d_in[1];
    const u16* b = (const u16*)d_in[2];
    (void)hipFuncSetAttribute((const void*)fused_kernel, hipFuncAttributeMaxDynamicSharedMemorySize, LDS_BYTES);
    fused_kernel<<<256, 512, LDS_BYTES, stream>>>(x, w, b, (char*)d_ws, (u16*)d_out);
}
```
